```python
import torch

def _softmax(x, axis=-1):
    return torch.softmax(x, dim=axis)
def _gelu(x):
    return torch.nn.functional.gelu(x, approximate="tanh")
def _silu(x):
    return torch.nn.functional.silu(x)
def _sigmoid(x):
    return torch.sigmoid(x)
def _layernorm(x, axis=-1, eps=1e-5):
    m = x.mean(dim=axis, keepdim=True)
    v = x.var(dim=axis, keepdim=True, unbiased=False)
    return (x - m) / torch.sqrt(v + eps)
def _scatter_add(tgt, idx, src, axis=0):
    return tgt.clone().index_add_(axis, idx, src)
def _segment_sum(src, seg, n):
    out = torch.zeros((n,) + src.shape[1:], dtype=src.dtype)
    return out.index_add_(0, seg, src)
def _pad(x, pw):
    flat = tuple(v for pair in reversed(pw) for v in pair)
    return torch.nn.functional.pad(x, flat)

kernel_name = 'gated_mix__spectral_fnet__glu_gated_glu__prompt'

def setup_inputs():

    _w = (lambda a: (lambda t: t.to(torch.bfloat16) if t.is_floating_point() else t)(torch.as_tensor(a)))
    def _A_setup():
        B, S, D = 1, 2048, 640
        ns = torch.arange(S)[:, None] * torch.arange(S)[None, :]
        nd = torch.arange(D)[:, None] * torch.arange(D)[None, :]
        Ws_re = torch.cos(-2.0 * torch.pi * ns / S).to(float)
        Ws_im = torch.sin(-2.0 * torch.pi * ns / S).to(float)
        Wd_re = torch.cos(-2.0 * torch.pi * nd / D).to(float)
        Wd_im = torch.sin(-2.0 * torch.pi * nd / D).to(float)
        return {
            "x": _w((lambda *s: torch.randn(*s, dtype=torch.bfloat16) * 0.05)(B, S, D)),
            "dft_seq_re": _w(Ws_re),
            "dft_seq_im": _w(Ws_im),
            "dft_hid_re": _w(Wd_re),
            "dft_hid_im": _w(Wd_im),
        }
    _A_in = _A_setup()

    def _B_setup():
        B, S, D, F = 1, 2048, 640, 1536
        return {
            "x": _w((lambda *s: torch.randn(*s, dtype=torch.bfloat16) * 0.05)(B, S, D)),
            "w_gate": _w((lambda *s: torch.randn(*s, dtype=torch.bfloat16) * 1.6)(D, F)),
            "w_up": _w((lambda *s: torch.randn(*s, dtype=torch.bfloat16) * 0.8)(D, F)),
            "w_down": _w((lambda *s: torch.randn(*s, dtype=torch.bfloat16) * 0.8)(F, D)),
        }
    _B_in = _B_setup()

    return {
        "A_x": _A_in["x"],
        "A_dft_seq_re": _A_in["dft_seq_re"],
        "A_dft_seq_im": _A_in["dft_seq_im"],
        "A_dft_hid_re": _A_in["dft_hid_re"],
        "A_dft_hid_im": _A_in["dft_hid_im"],
        "B_x": _B_in["x"],
        "B_w_gate": _B_in["w_gate"],
        "B_w_up": _B_in["w_up"],
        "B_w_down": _B_in["w_down"],
        "gate_w": _w((lambda *s: torch.randn(*s, dtype=torch.bfloat16) * 0.05)(640) * 16.0),
    }

def reference(A_x, A_dft_seq_re, A_dft_seq_im, A_dft_hid_re, A_dft_hid_im, B_x, B_w_gate, B_w_up, B_w_down, gate_w):
    def _A_ref(x, dft_seq_re, dft_seq_im, dft_hid_re, dft_hid_im):
        a = torch.einsum("bsd,ed->bse", x, dft_hid_re)
        b = torch.einsum("bsd,ed->bse", x, dft_hid_im)
        y = torch.einsum("ts,bse->bte", dft_seq_re, a) - torch.einsum("ts,bse->bte", dft_seq_im, b)
        return y

    def _B_ref(x, w_gate, w_up, w_down):
        g = torch.einsum("bsd,df->bsf", x, w_gate)
        u = torch.einsum("bsd,df->bsf", x, w_up)
        h = _sigmoid(g) * u
        return torch.einsum("bsf,fd->bsd", h, w_down)

    x = A_x
    g = _sigmoid((x * gate_w).sum(-1, keepdims=True))
    a = _A_ref(x, A_dft_seq_re, A_dft_seq_im, A_dft_hid_re, A_dft_hid_im)
    b = _B_ref(x, B_w_gate, B_w_up, B_w_down)
    return g * a + (1 - g) * b
```

```cpp
#include <hip/hip_runtime.h>
#include <stdint.h>
typedef __attribute__((ext_vector_type(8))) short bf16x8;
typedef __attribute__((ext_vector_type(4))) float f32x4;
typedef unsigned short u16;
__device__ __forceinline__ float bf2f(u16 v){ return __uint_as_float(((unsigned)v)<<16); }
__device__ __forceinline__ u16 f2bf(float f){ unsigned u=__float_as_uint(f); u += 0x7fff + ((u>>16)&1); return (u16)(u>>16); }
constexpr int S=2048, D=640, F=1536;

__global__ void transp(const u16* __restrict__ in, u16* __restrict__ out, int K, int N){
  __shared__ u16 t[32][33];
  int n0=blockIdx.x*32, k0=blockIdx.y*32;
  int tx=threadIdx.x&31, ty=threadIdx.x>>5;
  for(int i=ty;i<32;i+=8) t[i][tx]=in[(size_t)(k0+i)*N+n0+tx];
  __syncthreads();
  for(int i=ty;i<32;i+=8) out[(size_t)(n0+i)*K+k0+tx]=t[tx][i];
}
__global__ void gatek(const u16* __restrict__ x, const u16* __restrict__ gw, float* __restrict__ g){
  int r=blockIdx.x, l=threadIdx.x; float s=0;
  for(int d=l;d<D;d+=64) s+=bf2f(x[r*D+d])*bf2f(gw[d]);
  for(int o=32;o;o>>=1) s+=__shfl_xor(s,o);
  if(l==0) g[r]=1.f/(1.f+__expf(-s));
}
__device__ __forceinline__ void mm32(const u16* A,int lda,const u16* B,int ldb,int K,f32x4 (&acc)[2][2],int lane){
  const u16* a=A+(size_t)(lane&15)*lda+(lane>>4)*8;
  const u16* b=B+(size_t)(lane&15)*ldb+(lane>>4)*8;
  for(int k=0;k<K;k+=32){
    bf16x8 a0=*(const bf16x8*)(a+k), a1=*(const bf16x8*)(a+(size_t)16*lda+k);
    bf16x8 b0=*(const bf16x8*)(b+k), b1=*(const bf16x8*)(b+(size_t)16*ldb+k);
    acc[0][0]=__builtin_amdgcn_mfma_f32_16x16x32_bf16(a0,b0,acc[0][0],0,0,0);
    acc[0][1]=__builtin_amdgcn_mfma_f32_16x16x32_bf16(a0,b1,acc[0][1],0,0,0);
    acc[1][0]=__builtin_amdgcn_mfma_f32_16x16x32_bf16(a1,b0,acc[1][0],0,0,0);
    acc[1][1]=__builtin_amdgcn_mfma_f32_16x16x32_bf16(a1,b1,acc[1][1],0,0,0);
  }
}
__global__ void k_ab(const u16* x,const u16* Hr,const u16* Hi,u16* abT){
  int lane=threadIdx.x&63,w=threadIdx.x>>6;
  int m0=blockIdx.y*64+(w>>1)*32, n0=blockIdx.x*64+(w&1)*32;
  const u16* H = n0<D? Hr+(size_t)n0*D : Hi+(size_t)(n0-D)*D;
  float sg = n0<D?1.f:-1.f;
  f32x4 acc[2][2]={};
  mm32(x+(size_t)m0*D,D,H,D,D,acc,lane);
  for(int i=0;i<2;i++)for(int j=0;j<2;j++)for(int r=0;r<4;r++)
    abT[(size_t)(n0+j*16+(lane&15))*S+m0+i*16+(lane>>4)*4+r]=f2bf(sg*acc[i][j][r]);
}
__global__ void k_h(const u16* x,const u16* WgT,const u16* WuT,u16* h){
  int lane=threadIdx.x&63,w=threadIdx.x>>6;
  int m0=blockIdx.y*64+(w>>1)*32, n0=blockIdx.x*64+(w&1)*32;
  f32x4 ag[2][2]={}, au[2][2]={};
  mm32(x+(size_t)m0*D,D,WgT+(size_t)n0*D,D,D,ag,lane);
  mm32(x+(size_t)m0*D,D,WuT+(size_t)n0*D,D,D,au,lane);
  for(int i=0;i<2;i++)for(int j=0;j<2;j++)for(int r=0;r<4;r++)
    h[(size_t)(m0+i*16+(lane>>4)*4+r)*F+n0+j*16+(lane&15)]=f2bf(au[i][j][r]/(1.f+__expf(-ag[i][j][r])));
}
__global__ void k_out(const u16* Cr,const u16* Ci,const u16* abT,const u16* h,const u16* WdT,const float* g,u16* out){
  int lane=threadIdx.x&63,w=threadIdx.x>>6;
  int m0=blockIdx.y*64+(w>>1)*32, n0=blockIdx.x*64+(w&1)*32;
  f32x4 y[2][2]={}, o[2][2]={};
  mm32(Cr+(size_t)m0*S,S,abT+(size_t)n0*S,S,S,y,lane);
  mm32(Ci+(size_t)m0*S,S,abT+(size_t)(n0+D)*S,S,S,y,lane);
  mm32(h+(size_t)m0*F,F,WdT+(size_t)n0*F,F,F,o,lane);
  for(int i=0;i<2;i++)for(int j=0;j<2;j++)for(int r=0;r<4;r++){
    int row=m0+i*16+(lane>>4)*4+r; float gg=g[row];
    out[(size_t)row*D+n0+j*16+(lane&15)]=f2bf(gg*y[i][j][r]+(1.f-gg)*o[i][j][r]);
  }
}
extern "C" void kernel_launch(void* const* d_in, const int* in_sizes, int n_in, void* d_out, int out_size, void* d_ws, size_t ws_size, hipStream_t stream){
  const u16 *x=(const u16*)d_in[0],*Cr=(const u16*)d_in[1],*Ci=(const u16*)d_in[2],*Hr=(const u16*)d_in[3],*Hi=(const u16*)d_in[4];
  const u16 *Wg=(const u16*)d_in[6],*Wu=(const u16*)d_in[7],*Wd=(const u16*)d_in[8],*gw=(const u16*)d_in[9];
  char* ws=(char*)d_ws;
  u16* WgT=(u16*)(ws+(1<<20)); u16* WuT=WgT+D*F; u16* WdT=WuT+D*F; u16* abT=WdT+D*F; u16* h=abT+2*D*S; float* g=(float*)(h+S*F);
  transp<<<dim3(F/32,D/32),256,0,stream>>>(Wg,WgT,D,F);
  transp<<<dim3(F/32,D/32),256,0,stream>>>(Wu,WuT,D,F);
  transp<<<dim3(D/32,F/32),256,0,stream>>>(Wd,WdT,F,D);
  gatek<<<S,64,0,stream>>>(x,gw,g);
  k_ab<<<dim3(2*D/64,S/64),256,0,stream>>>(x,Hr,Hi,abT);
  k_h<<<dim3(F/64,S/64),256,0,stream>>>(x,WgT,WuT,h);
  k_out<<<dim3(D/64,S/64),256,0,stream>>>(Cr,Ci,abT,h,WdT,g,(u16*)d_out);
}
```

```cpp
#include <hip/hip_runtime.h>
#include <stdint.h>
typedef __attribute__((ext_vector_type(8))) short bf16x8;
typedef __attribute__((ext_vector_type(4))) short bf16x4;
typedef __attribute__((ext_vector_type(4))) float f32x4;
typedef __attribute__((ext_vector_type(2))) float f32x2;
typedef __attribute__((ext_vector_type(2))) __bf16 bfx2;
typedef __attribute__((ext_vector_type(4))) unsigned u32x4;
typedef __attribute__((ext_vector_type(2))) unsigned u32x2;
typedef unsigned short u16;
#define GAS __attribute__((address_space(1)))
#define LAS __attribute__((address_space(3)))
#define DI __device__ __forceinline__

constexpr int SLOT = 40960;
constexpr int LDS_BYTES = 4 * SLOT;
constexpr unsigned WS_E  = 0;
constexpr unsigned WS_FA = 1u << 16;
constexpr unsigned WS_FH = 2u << 16;
constexpr unsigned WS_FP = 4u << 16;
constexpr unsigned WS_G  = 8u << 16;
constexpr unsigned WS_AB = 1u << 20;
constexpr unsigned WS_H  = 8u << 20;
constexpr unsigned WS_P  = 20u << 20;
constexpr unsigned WS_END = 32u << 20;

#define CPOL 0
#define ROTM 15
#define PUB1 1
#define PUB2 1
#define DMAP(g, l, off, pol) __builtin_amdgcn_global_load_lds((const GAS void*)(g), (LAS void*)(l), 16, off, pol)
#define DMA(g, l, off) DMAP(g, l, off, 0)
#define DMAC(g, l, off) DMAP(g, l, off, 16)
#define WAITV(n) asm volatile("s_waitcnt vmcnt(" #n ")" ::: "memory")
#define BAR() do { asm volatile("" ::: "memory"); __builtin_amdgcn_s_barrier(); asm volatile("" ::: "memory"); } while (0)
#define LGKM0() __builtin_amdgcn_s_waitcnt(0xC07F)
#define MFMA(a, b, c) __builtin_amdgcn_mfma_f32_16x16x32_bf16(a, b, c, 0, 0, 0)
#define SGB(mask, n, id) __builtin_amdgcn_sched_group_barrier(mask, n, id)
#define SB0() __builtin_amdgcn_sched_barrier(0)

DI unsigned pk2(float a, float b) { f32x2 v = {a, b}; return __builtin_bit_cast(unsigned, __builtin_convertvector(v, bfx2)); }
DI float bflo(unsigned v) { return __uint_as_float(v << 16); }
DI float bfhi(unsigned v) { return __uint_as_float(v & 0xffff0000u); }
DI float sigm(float v) { return __builtin_amdgcn_rcpf(1.f + __expf(-v)); }
DI bf16x8 lds128(unsigned a) { return *(LAS bf16x8*)a; }
DI bf16x4 ldstr(unsigned a) { return __builtin_amdgcn_ds_read_tr16_b64_v4i16((LAS bf16x4*)a); }
DI bf16x8 cat(bf16x4 a, bf16x4 b) { return __builtin_shufflevector(a, b, 0, 1, 2, 3, 4, 5, 6, 7); }
typedef __amdgpu_buffer_rsrc_t rsrc_t;
DI rsrc_t mkrs(const void* p, unsigned bytes) { return __builtin_amdgcn_make_buffer_rsrc((void*)p, 0, bytes, 0x00020000); }
DI void st16(rsrc_t ws, unsigned off, u32x4 v) { __builtin_amdgcn_raw_buffer_store_b128(v, ws, off, 0, 16); }
DI void st8(rsrc_t ws, unsigned off, u32x2 v) { __builtin_amdgcn_raw_buffer_store_b64(v, ws, off, 0, 16); }
#define LD8(d, ws, off) asm volatile("global_load_dwordx2 %0, %1, %2 sc1" : "=v"(d) : "v"(off), "s"(ws) : "memory")
#define LD16(d, ws, off, imm) asm volatile("global_load_dwordx4 %0, %1, %2 offset:" #imm " sc1" : "=v"(d) : "v"(off), "s"(ws) : "memory")
DI void spin(u32x2 v, char* ws, unsigned off, unsigned t0, unsigned t1) {
  for (int i = 0; i < (1 << 18); i++) {
    if (__builtin_amdgcn_ballot_w64(v[0] != t0 || v[1] != t1) == 0) break;
    __builtin_amdgcn_s_sleep(1);
    LD8(v, ws, off);
    asm volatile("s_waitcnt vmcnt(0)" : "+v"(v) :: "memory");
  }
}

#define TS(i)
#define TW(i)
#define STAMP(i)
#define STAMP_DECL
#define STAMP_OUT()
#define STAMP_ARG

__global__ __launch_bounds__(512, 2) void kf(const u16* __restrict__ x, const u16* __restrict__ Hr, const u16* __restrict__ Hi,
                                             const u16* __restrict__ Wg, const u16* __restrict__ Wu, const u16* __restrict__ Wd,
                                             const u16* __restrict__ gw, const u16* __restrict__ Cr, const u16* __restrict__ Ci,
                                             char* __restrict__ ws, u16* __restrict__ out STAMP_ARG) {
  extern __shared__ __attribute__((aligned(16))) char smem[];
  const int tid = threadIdx.x, lane = tid & 63;
  const int wid = __builtin_amdgcn_readfirstlane(tid >> 6);
  const int b = blockIdx.x, xcd = b & 7, loc = b >> 3;
  const int mi = (xcd >> 2) * 8 + (loc >> 2), kq = xcd & 3, nt = loc & 3, nj = kq * 4 + nt;
  const unsigned sm = (unsigned)(size_t)(LAS char*)smem;
  const int r = lane & 15, q = lane >> 4;
  STAMP_DECL; STAMP(0);
  const rsrc_t wr_ = mkrs(ws, WS_END);
  if (wid >= 4) {
    const int w = wid - 4;
    const char* xb = (const char*)x + (size_t)(128 * mi + 8 * w) * 1280;
    const unsigned vx1 = (lane >> 3) * 1280 + (((lane & 7) ^ ((lane >> 4) & 1) ^ (w << 1)) << 4);
    const char* hb = (const char*)(nj >= 8 ? Hi : Hr) + (size_t)(80 * (nj & 7)) * 1280;
    const int hrow = 20 * w + (lane >> 3);
    const unsigned vh0 = hrow * 1280 + (((lane & 7) ^ ((hrow >> 1) & 7)) << 4);
    const unsigned vh1 = (hrow + 8) * 1280 + (((lane & 7) ^ (((hrow + 8) >> 1) & 7)) << 4);
    const char* ph2 = lane < 32 ? hb + vh0 + 16 * 1280 : (const char*)gw + (lane & 7) * 16;
    const bool h2on = lane < (w == 3 ? 40 : 32);
    const int rotk = ((loc >> 2) + ((5 * (loc & 3)) >> 1)) % 10;
    auto issue1 = [&](int t, int T) {
      if (ROTM & 1) { t += rotk; if (t >= 10) t -= 10; }
      const unsigned sl = sm + (T & 3) * SLOT;
      const char* xa = xb + t * 128;
#pragma unroll
      for (int j = 0; j < 4; j++) DMA(xa + j * 40960 + vx1, sl + (w + 4 * j) * 1024, 0);
      const char* ha = hb + t * 128;
      const unsigned hl = sl + 16384 + w * 2560;
      DMA(ha + vh0, hl, 0);
      DMA(ha + vh1, hl + 1024, 0);
      if (h2on) DMA(ph2 + t * 128, hl + 2048, 0);
    };
    issue1(0, 0); issue1(1, 1);
    u32x2 ep; LD8(ep, ws, 0);
    STAMP(1);
    const unsigned vx2 = (lane >> 3) * 1280 + (((lane & 7) ^ (lane >> 4) ^ (4 * (w & 1))) << 4);
    const int fa = (nj & 1) ? 32 : 0, fb = (nj & 1) ? 0 : 64;
    const int sg = ((lane >> 4) & 1) | ((w & 1) << 1);
    const int gc = (lane & 7) ^ (sg << 1);
    const unsigned vw = (lane >> 3) * 3072 + (gc << 4);
    const char* gab = (const char*)Wg + (size_t)(8 * w) * 3072 + (96 * nj + fa) * 2;
    const char* uab = (const char*)Wu + (size_t)(8 * w) * 3072 + (96 * nj + fa) * 2;
    const char* pB = (const char*)(gc < 4 ? Wg : Wu) + (size_t)(8 * w + (lane >> 3)) * 3072 + (96 * nj + fb) * 2 + ((gc & 3) << 4);
    auto issue2 = [&](int t, int T) {
      if (ROTM & 2) { t += rotk; if (t >= 10) t -= 10; }
      const unsigned sl = sm + (T & 3) * SLOT;
      const char* xa = xb + t * 128;
#pragma unroll
      for (int j = 0; j < 4; j++) DMA(xa + j * 40960 + vx2, sl + (w + 4 * j) * 1024, 0);
      const size_t ko = (size_t)t * (64 * 3072);
#pragma unroll
      for (int j = 0; j < 2; j++) {
        DMA(gab + ko + j * (32 * 3072) + vw, sl + 16384 + (w + 4 * j) * 1024, 0);
        DMA(uab + ko + j * (32 * 3072) + vw, sl + 24576 + (w + 4 * j) * 1024, 0);
        DMA(pB + ko + j * (32 * 3072), sl + 32768 + (w + 4 * j) * 1024, 0);
      }
    };
    const char* cb = (const char*)((kq >> 1) ? Ci : Cr) + (size_t)(128 * mi + 8 * w) * 4096 + (kq & 1) * 2048;
    const unsigned va = (lane >> 3) * 4096 + (((lane & 7) ^ (lane >> 4) ^ (4 * (w & 1))) << 4);
    const char* bb = ws + WS_AB + (size_t)(((kq >> 1) * 64 + 32 * (kq & 1) + (w >> 1)) * 40 + 10 * nt + 5 * (w & 1)) * 1024 + 2048;
    const unsigned vl = lane * 16;
    auto issue3 = [&](int t) {
      const unsigned sl = sm + (t & 3) * SLOT;
      if (ROTM & 4) t = (t + 4 * nt + 2 * (loc >> 2)) & 15;
      const char* ca = cb + t * 128;
#pragma unroll
      for (int j = 0; j < 4; j++) DMAP(ca + j * 131072 + va, sl + (w + 4 * j) * 1024, 0, CPOL);
      const char* ba = bb + (size_t)t * 81920 + vl;
      const unsigned sb = sl + 16384 + w * 5120 + 2048;
      DMAC(ba, sb, -2048); DMAC(ba, sb, -1024); DMAC(ba, sb, 0); DMAC(ba, sb, 1024); DMAC(ba, sb, 2048);
    };
    const char* hh = ws + WS_H + (size_t)((8 * mi + 2 * w) * 48 + 12 * kq) * 1024;
    const char* db = (const char*)Wd + (size_t)(384 * kq + 16 * w) * 1280 + 320 * nt;
    unsigned vd[5];
#pragma unroll
    for (int i = 0; i < 5; i++) {
      const int idx = i * 64 + lane, row = idx / 20, c = idx - row * 20;
      vd[i] = row * 1280 + ((c ^ ((row >> 2) & 2)) << 4);
    }
    const int rot4 = (nt + (loc >> 2) + (nt >> 1)) % 6;
    auto issue4 = [&](int t) {
      const unsigned sl = sm + (t & 3) * SLOT;
      if (ROTM & 8) { t += rot4; if (t >= 6) t -= 6; }
      const char* ha = hh + t * 2048 + vl;
      const unsigned sh = sl + w * 4096;
      DMAC(ha, sh, 0); DMAC(ha, sh, 1024);
      DMAC(ha + 49152 - 2048, sh, 2048); DMAC(ha + 49152 - 2048, sh, 3072);
      const char* da = db + t * (64 * 1280);
      const unsigned sb = sl + 16384 + w * 5120;
#pragma unroll
      for (int i = 0; i < 5; i++) DMA(da + vd[i], sb + i * 1024, 0);
    };
    const unsigned ofa = WS_FA + ((8 * (kq & 1) + (lane & 7)) * 16 + 8 * (kq >> 1) + 2 * nt + ((lane >> 3) & 1)) * 128;
    const unsigned ofh = WS_FH + (mi * 16 + 4 * kq + (lane & 3)) * 128;
    WAITV(8); STAMP(2); BAR();
    issue1(2, 2); WAITV(8);
    BAR(); issue1(3, 3); issue1(4, 4);
    asm volatile("s_waitcnt vmcnt(14)" : "+v"(ep) :: "memory");
    const unsigned t0 = __builtin_amdgcn_readfirstlane(ep[0]) * 1664525u + 1013904223u;
    const unsigned t1 = __builtin_amdgcn_readfirstlane(ep[1]) * 22695477u + 1u;
    for (int T = 1; T < 6; T++) { BAR(); TS(T); issue1(T + 4, T + 4); WAITV(14); TW(T); }
    BAR(); TS(6); issue2(0, 10); WAITV(17); TW(6);
    for (int T = 7; T < 15; T++) { BAR(); TS(T); issue2(T - 6, T + 4); WAITV(20); TW(T); }
    u32x2 fv;
    BAR(); TS(15); LD8(fv, ws, ofa); issue2(9, 19); WAITV(21); TW(15);
    BAR(); TS(16); asm volatile("s_waitcnt vmcnt(10)" : "+v"(fv) :: "memory");
    STAMP(3); spin(fv, ws, ofa, t0, t1); STAMP(4);
    issue3(0); WAITV(19); TW(16);
    for (int T = 17; T < 31; T++) { BAR(); TS(T); issue3(T - 16); WAITV(18); TW(T); }
    BAR(); TS(31); LD8(fv, ws, ofh); issue3(15); WAITV(19); TW(31);
    BAR(); TS(32); asm volatile("s_waitcnt vmcnt(9)" : "+v"(fv) :: "memory");
    STAMP(5); spin(fv, ws, ofh, t0, t1); STAMP(6);
    issue4(0); WAITV(18); TW(32);
    for (int T = 33; T < 38; T++) { BAR(); TS(T); issue4(T - 32); WAITV(18); TW(T); }
    BAR(); TS(38); WAITV(9); TW(38);
    BAR(); TS(39); WAITV(0); TW(39);
    BAR(); TS(40);
    if (b == 0) {
      const unsigned oa = WS_FA + (w * 64 + lane) * 128;
      LD8(fv, ws, oa); asm volatile("s_waitcnt vmcnt(0)" : "+v"(fv) :: "memory");
      spin(fv, ws, oa, t0, t1);
    }
    BAR();
    if (b == 0 && w == 0 && lane == 0) st8(wr_, WS_E, u32x2{t0, t1});
    STAMP(7); STAMP_OUT();
  } else {
    const int w = wid, wr = w >> 1, wc = w & 1;
    const int z = (r >> 1) & 7;
    u32x2 ep; LD8(ep, ws, 0);
    unsigned t0, t1;
    {
      f32x4 acc[2][6];
#pragma unroll
      for (int i = 0; i < 2; i++)
#pragma unroll
        for (int j = 0; j < 6; j++) acc[i][j] = f32x4{0, 0, 0, 0};
      const unsigned ax = sm + (8 * (r >> 2) + (r & 3)) * 128 + ((q ^ z) << 4) + 4096 * w;
      const unsigned ah = sm + 16384 + r * 128 + ((q ^ z) << 4), ag = sm + 16384 + 10240 + (q << 4);
      bf16x8 a0[2], b0[6], a1[2], b1[6];
      auto rd = [&](bf16x8 (&a)[2], bf16x8 (&bb)[6], int T, int h) {
        const unsigned so = (T & 3) * SLOT, hx = h * 64;
        a[0] = lds128((ax ^ hx) + so); a[1] = lds128((ax ^ hx) + so + 512);
#pragma unroll
        for (int j = 0; j < 5; j++) bb[j] = lds128((ah ^ hx) + so + j * 2048);
        bb[5] = lds128(ag + hx + so);
      };
      auto mm = [&](bf16x8 (&a)[2], bf16x8 (&bb)[6]) {
#pragma unroll
        for (int j = 0; j < 6; j++)
#pragma unroll
          for (int i = 0; i < 2; i++) acc[i][j] = MFMA(a[i], bb[j], acc[i][j]);
      };
      asm volatile("s_waitcnt vmcnt(0)" : "+v"(ep) :: "memory");
      t0 = __builtin_amdgcn_readfirstlane(ep[0]) * 1664525u + 1013904223u;
      t1 = __builtin_amdgcn_readfirstlane(ep[1]) * 22695477u + 1u;
      BAR();
      STAMP(1);
      rd(a0, b0, 0, 0);
      for (int t = 0; t < 10; t++) {
        rd(a1, b1, t, 1);
        mm(a0, b0);
#pragma unroll
        for (int i = 0; i < 8; i++) { SGB(0x008, 1, 0); SGB(0x100, 1, 0); }
        SGB(0x008, 4, 0);
        SB0();
        LGKM0(); BAR();
        SB0();
        if (t < 9) rd(a0, b0, t + 1, 0);
        mm(a1, b1);
#pragma unroll
        for (int i = 0; i < 8; i++) { SGB(0x008, 1, 1); SGB(0x100, 1, 1); }
        SGB(0x008, 4, 1);
        SB0();
      }
      STAMP(2);
      const float sgn = nj >= 8 ? -1.f : 1.f;
      const unsigned dst = WS_AB + ((((nj >> 3) * 64 + 4 * mi + w) * 40) + 5 * (nj & 7)) * 1024 + lane * 16;
#pragma unroll
      for (int j = 0; j < 5; j++) {
        u32x4 o = {pk2(sgn * acc[0][j][0], sgn * acc[0][j][1]), pk2(sgn * acc[0][j][2], sgn * acc[0][j][3]),
                   pk2(sgn * acc[1][j][0], sgn * acc[1][j][1]), pk2(sgn * acc[1][j][2], sgn * acc[1][j][3])};
        st16(wr_, dst + j * 1024, o);
      }
      const f32x4 gl = (r & 1) ? acc[1][5] : acc[0][5];
      if (r < 2) *(GAS f32x4*)(ws + WS_G + b * 512 + (32 * w + 8 * q + 4 * r) * 4) = gl;
    }
    float gv[4];
    {
      f32x4 ag[3][4], au[3][4];
#pragma unroll
      for (int c = 0; c < 3; c++)
#pragma unroll
        for (int i = 0; i < 4; i++) { ag[c][i] = f32x4{0, 0, 0, 0}; au[c][i] = f32x4{0, 0, 0, 0}; }
      const unsigned ax = sm + (64 * wr + r) * 128 + ((q ^ z) << 4);
      const int qq = r >> 2, p = lane & 3, sg = ((qq >> 1) & 1) | ((q & 1) << 1);
      const unsigned tb = sm + (8 * q + qq) * 128 + 8 * p;
      const unsigned tA0 = tb + (((2 * wc) ^ sg) << 5) + 16384, tA1 = tA0 ^ 32;
      const unsigned tBg = tb + ((wc ^ sg) << 5) + 32768, tBu = tBg ^ 64;
      bf16x8 x0[4], g0[3], u0[3], x1[4], g1[3], u1[3];
      auto rd = [&](bf16x8 (&xx)[4], bf16x8 (&g)[3], bf16x8 (&u)[3], int T, int h) {
        const unsigned so = (T & 3) * SLOT, hx = h * 64, ho = so + h * 4096;
        g[0] = cat(ldstr(tA0 + ho), ldstr(tA0 + ho + 512));
#pragma unroll
        for (int i = 0; i < 4; i++) xx[i] = lds128((ax ^ hx) + so + i * 2048);
        u[0] = cat(ldstr(tA0 + ho + 8192), ldstr(tA0 + ho + 8192 + 512));
        g[1] = cat(ldstr(tA1 + ho), ldstr(tA1 + ho + 512));
        u[1] = cat(ldstr(tA1 + ho + 8192), ldstr(tA1 + ho + 8192 + 512));
        g[2] = cat(ldstr(tBg + ho), ldstr(tBg + ho + 512));
        u[2] = cat(ldstr(tBu + ho), ldstr(tBu + ho + 512));
      };
      auto mm = [&](bf16x8 (&xx)[4], bf16x8 (&g)[3], bf16x8 (&u)[3]) {
#pragma unroll
        for (int c = 0; c < 3; c++) {
#pragma unroll
          for (int i = 0; i < 4; i++) ag[c][i] = MFMA(g[c], xx[i], ag[c][i]);
#pragma unroll
          for (int i = 0; i < 4; i++) au[c][i] = MFMA(u[c], xx[i], au[c][i]);
        }
      };
      const unsigned go = WS_G + b * 512 + (64 * wr + r) * 4;
      rd(x0, g0, u0, 10, 0);
      for (int t = 0; t < 10; t++) {
        rd(x1, g1, u1, t + 10, 1);
        mm(x0, g0, u0);
#pragma unroll
        for (int i = 0; i < 16; i++) { SGB(0x008, 1, 0); SGB(0x100, 1, 0); }
        SGB(0x008, 8, 0);
        SB0();
        if (t == PUB1) WAITV(0);
        LGKM0(); BAR();
        if (t == PUB1) {
          if (w == 0 && lane == 0) st8(wr_, WS_FA + (mi * 16 + nj) * 128, u32x2{t0, t1});
          asm volatile("global_load_dword %0, %4, %5\n\tglobal_load_dword %1, %4, %5 offset:64\n\tglobal_load_dword %2, %4, %5 offset:128\n\tglobal_load_dword %3, %4, %5 offset:192"
                       : "=&v"(gv[0]), "=&v"(gv[1]), "=&v"(gv[2]), "=&v"(gv[3]) : "v"(go), "s"(ws) : "memory");
        }
        SB0();
        if (t < 9) rd(x0, g0, u0, t + 11, 0);
        mm(x1, g1, u1);
#pragma unroll
        for (int i = 0; i < 16; i++) { SGB(0x008, 1, 1); SGB(0x100, 1, 1); }
        SGB(0x008, 8, 1);
        SB0();
      }
      STAMP(3);
      asm volatile("s_waitcnt vmcnt(0)" : "+v"(gv[0]), "+v"(gv[1]), "+v"(gv[2]), "+v"(gv[3]) :: "memory");
      float og[4];
#pragma unroll
      for (int i = 0; i < 4; i++) { gv[i] = sigm(gv[i]); og[i] = 1.f - gv[i]; }
      const int fa16 = (nj & 1) ? 2 : 0, fb16 = (nj & 1) ? 0 : 4;
      const unsigned dst = WS_H + ((8 * mi + 4 * wr + (q & 1)) * 48 + 3 * nj) * 1024 + ((q >> 1) * 16 + r) * 16;
#pragma unroll
      for (int c = 0; c < 3; c++) {
        const int blk = c < 2 ? fa16 + 2 * wc + c : fb16 + wc;
        const unsigned dc = dst + (blk >> 1) * 1024 + (blk & 1) * 512;
#pragma unroll
        for (int i = 0; i < 4; i += 2) {
          unsigned e[2][2];
#pragma unroll
          for (int k = 0; k < 2; k++)
#pragma unroll
            for (int d = 0; d < 2; d++)
              e[k][d] = pk2(au[c][i + k][2 * d] * og[i + k] * sigm(ag[c][i + k][2 * d]), au[c][i + k][2 * d + 1] * og[i + k] * sigm(ag[c][i + k][2 * d + 1]));
          auto s0 = __builtin_amdgcn_permlane16_swap(e[0][0], e[1][0], false, false);
          auto s1 = __builtin_amdgcn_permlane16_swap(e[0][1], e[1][1], false, false);
          st16(wr_, dc + i * 49152, u32x4{s0[0], s1[0], s0[1], s1[1]});
        }
      }
      STAMP(4);
    }
    f32x4 acc[4][5];
#pragma unroll
    for (int i = 0; i < 4; i++)
#pragma unroll
      for (int j = 0; j < 5; j++) acc[i][j] = f32x4{0, 0, 0, 0};
    bf16x8 a0[4], b0[5], a1[4], b1[5];
    auto mm = [&](bf16x8 (&a)[4], bf16x8 (&bb)[5]) {
#pragma unroll
      for (int j = 0; j < 5; j++)
#pragma unroll
        for (int i = 0; i < 4; i++) acc[i][j] = MFMA(bb[j], a[i], acc[i][j]);
    };
    const unsigned ah4 = sm + wr * 8192 + lane * 16;
    const unsigned tde = sm + 16384 + (8 * q + (r >> 2)) * 320 + 8 * (lane & 3) + 160 * wc + ((q & 1) ? (wc ? -32 : 32) : 0);
    const unsigned tdo = sm + 16384 + (8 * q + (r >> 2)) * 320 + 8 * (lane & 3) + 160 * wc + ((q & 1) ? (wc ? 32 : -32) : 0);
    auto rd4 = [&](bf16x8 (&a)[4], bf16x8 (&bb)[5], int T, int h) {
      const unsigned so = (T & 3) * SLOT, ho = so + h * 10240;
      bb[0] = cat(ldstr(tde + ho), ldstr(tde + ho + 1280));
#pragma unroll
      for (int i = 0; i < 4; i++) a[i] = lds128(ah4 + so + h * 1024 + i * 2048);
#pragma unroll
      for (int j = 1; j < 5; j++) bb[j] = cat(ldstr(((j & 1) ? tdo : tde) + ho + 32 * j), ldstr(((j & 1) ? tdo : tde) + ho + 32 * j + 1280));
    };
    {
      const unsigned aa = sm + (64 * wr + r) * 128 + ((q ^ z) << 4);
      const unsigned ab = sm + 16384 + wc * 5120 + lane * 16;
      auto rd = [&](bf16x8 (&a)[4], bf16x8 (&bb)[5], int T, int h) {
        const unsigned so = (T & 3) * SLOT, hx = h * 64;
        bb[0] = lds128(ab + so + h * 10240);
#pragma unroll
        for (int i = 0; i < 4; i++) a[i] = lds128((aa ^ hx) + so + i * 2048);
#pragma unroll
        for (int j = 1; j < 5; j++) bb[j] = lds128(ab + so + h * 10240 + j * 1024);
      };
      rd(a0, b0, 0, 0);
      for (int t = 0; t < 16; t++) {
        rd(a1, b1, t, 1);
        mm(a0, b0);
#pragma unroll
        for (int i = 0; i < 9; i++) { SGB(0x008, 2, 0); SGB(0x100, 1, 0); }
        SGB(0x008, 2, 0);
        SB0();
        if (t == PUB2) WAITV(0);
        LGKM0(); BAR();
        if (t == PUB2 && w == 0 && lane == 0) st8(wr_, WS_FH + (mi * 16 + nj) * 128, u32x2{t0, t1});
        SB0();
        if (t < 15) rd(a0, b0, t + 1, 0); else rd4(a0, b0, 0, 0);
        mm(a1, b1);
#pragma unroll
        for (int i = 0; i < 9; i++) { SGB(0x008, 2, 1); SGB(0x100, 1, 1); }
        SGB(0x008, 2, 1);
        SB0();
      }
      STAMP(5);
#pragma unroll
      for (int j = 0; j < 5; j++)
#pragma unroll
        for (int i = 0; i < 4; i++) acc[i][j] *= gv[i];
    }
    {
      for (int t = 0; t < 6; t++) {
        rd4(a1, b1, t, 1);
        mm(a0, b0);
#pragma unroll
        for (int i = 0; i < 14; i++) { SGB(0x008, 1, 0); SGB(0x100, 1, 0); }
        SGB(0x008, 6, 0);
        SB0();
        if (t < 5) { LGKM0(); BAR(); }
        SB0();
        if (t < 5) rd4(a0, b0, t + 1, 0);
        mm(a1, b1);
#pragma unroll
        for (int i = 0; i < 14; i++) { SGB(0x008, 1, 1); SGB(0x100, 1, 1); }
        SGB(0x008, 6, 1);
        SB0();
      }
      STAMP(6);
    }
    const int grp = mi * 4 + nt;
    const unsigned own = sm + 2 * SLOT + lane * 16;
    if (w == kq) {
#pragma unroll
      for (int i = 0; i < 4; i++)
#pragma unroll
        for (int j = 0; j < 5; j++) *(LAS f32x4*)(own + (i * 5 + j) * 1024) = acc[i][j];
    } else {
      const unsigned po = WS_P + ((grp * 4 + w) * 4 + kq) * 10240 + lane * 16;
#pragma unroll
      for (int i = 0; i < 4; i++) {
        st16(wr_, po + i * 2048, u32x4{pk2(acc[i][0][0], acc[i][0][1]), pk2(acc[i][0][2], acc[i][0][3]), pk2(acc[i][1][0], acc[i][1][1]), pk2(acc[i][1][2], acc[i][1][3])});
        st16(wr_, po + i * 2048 + 1024, u32x4{pk2(acc[i][2][0], acc[i][2][1]), pk2(acc[i][2][2], acc[i][2][3]), pk2(acc[i][3][0], acc[i][3][1]), pk2(acc[i][3][2], acc[i][3][3])});
      }
#pragma unroll
      for (int i = 0; i < 4; i += 2)
        st16(wr_, po + 8192 + i * 512, u32x4{pk2(acc[i][4][0], acc[i][4][1]), pk2(acc[i][4][2], acc[i][4][3]), pk2(acc[i + 1][4][0], acc[i + 1][4][1]), pk2(acc[i + 1][4][2], acc[i + 1][4][3])});
      WAITV(0);
      if (lane == 0) st8(wr_, WS_FP + ((grp * 4 + w) * 4 + kq) * 128, u32x2{t0, t1});
    }
    STAMP(7);
    LGKM0(); BAR();
    {
      const int src = (kq + 1 + (lane & 3) % 3) & 3;
      const unsigned ofp = WS_FP + ((grp * 4 + kq) * 4 + src) * 128;
      u32x2 fv; LD8(fv, ws, ofp);
      f32x4 y[5];
#pragma unroll
      for (int j = 0; j < 5; j++) y[j] = *(LAS f32x4*)(own + (w * 5 + j) * 1024);
      asm volatile("s_waitcnt vmcnt(0)" : "+v"(fv) :: "memory");
      STAMP(8);
      spin(fv, ws, ofp, t0, t1);
      STAMP(9);
      u32x4 p0[3], p1[3]; u32x2 p2[3];
#pragma unroll
      for (int s = 0; s < 3; s++) {
        const unsigned pi = WS_P + ((grp * 4 + kq) * 4 + ((kq + 1 + s) & 3)) * 10240;
        LD16(p0[s], ws, pi + w * 2048 + lane * 16, 0); LD16(p1[s], ws, pi + w * 2048 + lane * 16, 1024);
        LD8(p2[s], ws, pi + 8192 + (w >> 1) * 1024 + lane * 16 + (w & 1) * 8);
      }
      asm volatile("s_waitcnt vmcnt(0)" : "+v"(p0[0]), "+v"(p0[1]), "+v"(p0[2]), "+v"(p1[0]), "+v"(p1[1]), "+v"(p1[2]), "+v"(p2[0]), "+v"(p2[1]), "+v"(p2[2]) :: "memory");
      STAMP(10);
#pragma unroll
      for (int s = 0; s < 3; s++) {
#pragma unroll
        for (int c = 0; c < 2; c++) {
          y[0][2 * c] += bflo(p0[s][c]); y[0][2 * c + 1] += bfhi(p0[s][c]); y[1][2 * c] += bflo(p0[s][2 + c]); y[1][2 * c + 1] += bfhi(p0[s][2 + c]);
          y[2][2 * c] += bflo(p1[s][c]); y[2][2 * c + 1] += bfhi(p1[s][c]); y[3][2 * c] += bflo(p1[s][2 + c]); y[3][2 * c + 1] += bfhi(p1[s][2 + c]);
          y[4][2 * c] += bflo(p2[s][c]); y[4][2 * c + 1] += bfhi(p2[s][c]);
        }
      }
      char* dst = (char*)out + (size_t)(128 * mi + 64 * (kq >> 1) + 16 * w + r) * 1280 + (160 * nt + 80 * (kq & 1)) * 2 + q * 8;
#pragma unroll
      for (int j = 0; j < 5; j++) *(GAS u32x2*)(dst + j * 32) = u32x2{pk2(y[j][0], y[j][1]), pk2(y[j][2], y[j][3])};
      STAMP(11); STAMP_OUT();
    }
  }
}

extern "C" void kernel_launch(void* const* d_in, const int* in_sizes, int n_in, void* d_out, int out_size, void* d_ws, size_t ws_size,
                              hipStream_t stream) {
  (void)hipFuncSetAttribute((const void*)kf, hipFuncAttributeMaxDynamicSharedMemorySize, LDS_BYTES);
  kf<<<256, 512, LDS_BYTES, stream>>>((const u16*)d_in[0], (const u16*)d_in[3], (const u16*)d_in[4], (const u16*)d_in[6], (const u16*)d_in[7],
                                      (const u16*)d_in[8], (const u16*)d_in[9], (const u16*)d_in[1], (const u16*)d_in[2], (char*)d_ws, (u16*)d_out);
}
```

```cpp
#include <hip/hip_runtime.h>
#include <stdint.h>
typedef __attribute__((ext_vector_type(8))) short bf16x8;
typedef __attribute__((ext_vector_type(4))) short bf16x4;
typedef __attribute__((ext_vector_type(4))) float f32x4;
typedef __attribute__((ext_vector_type(2))) float f32x2;
typedef __attribute__((ext_vector_type(2))) __bf16 bfx2;
typedef __attribute__((ext_vector_type(4))) unsigned u32x4;
typedef __attribute__((ext_vector_type(2))) unsigned u32x2;
typedef unsigned short u16;
#define GAS __attribute__((address_space(1)))
#define LAS __attribute__((address_space(3)))
#define DI __device__ __forceinline__

constexpr int SLOT = 40960;
constexpr int LDS_BYTES = 4 * SLOT;
constexpr int P1B = 52736, P1S = 26752;
constexpr int CE = 3 * SLOT;
constexpr unsigned WS_E  = 0;
constexpr unsigned WS_FA = 1u << 16;
constexpr unsigned WS_FH = 2u << 16;
constexpr unsigned WS_G  = 8u << 16;
constexpr unsigned WS_AB = 1u << 20;
constexpr unsigned WS_H  = 8u << 20;
constexpr unsigned WS_P  = 20u << 20;
constexpr unsigned WS_END = 40u << 20;

#define CPOL 0
#define ROTM 14
#define PWM 0
#define PCM 0
#define PUB1 2
#define PUB2 8
#define XP 0
#define PRIO 0
#define POLL 1
#define POLLK 22
#define CPF 0
#define DMAP(g, l, off, pol) __builtin_amdgcn_global_load_lds((const GAS void*)(g), (LAS void*)(l), 16, off, pol)
#define DMA(g, l, off) DMAP(g, l, off, 0)
#define DMAC(g, l, off) DMAP(g, l, off, 16)
#define WAITV(n) asm volatile("s_waitcnt vmcnt(" #n ")" ::: "memory")
#define BAR() do { asm volatile("" ::: "memory"); __builtin_amdgcn_s_barrier(); asm volatile("" ::: "memory"); } while (0)
#define LGKM0() __builtin_amdgcn_s_waitcnt(0xC07F)
#define MFMAT(a, b, c) do { asm volatile("v_mfma_f32_16x16x32_bf16 %0, %1, %2, %0" : "+v"(c) : "v"(a), "v"(b)); __builtin_amdgcn_sched_barrier(0); } while (0)
#define MFMAZ(a, b, c) do { asm volatile("v_mfma_f32_16x16x32_bf16 %0, %1, %2, 0" : "=&v"(c) : "v"(a), "v"(b)); __builtin_amdgcn_sched_barrier(0); } while (0)
#define OPQ(v) asm volatile("" : "+v"(v))
#define UNR _Pragma("unroll")
#define INL __attribute__((always_inline))
#define SB0() __builtin_amdgcn_sched_barrier(0)
template <int N> struct IC { static constexpr int v = N; };
template <int I, int N, class F> DI void sfor(F&& f) { if constexpr (I < N) { f(IC<I>{}); sfor<I + 1, N>(f); } }
#define SF(var, n) sfor<0, n>([&](auto var##_c) INL { constexpr int var = decltype(var##_c)::v;
#define SE });

DI unsigned pk2(float a, float b) { f32x2 v = {a, b}; return __builtin_bit_cast(unsigned, __builtin_convertvector(v, bfx2)); }
DI float bflo(unsigned v) { return __uint_as_float(v << 16); }
DI float bfhi(unsigned v) { return __uint_as_float(v & 0xffff0000u); }
DI unsigned pkh(float a, float b) { return __builtin_bit_cast(unsigned, __builtin_amdgcn_cvt_pkrtz(a, b)); }
DI float mixlo(unsigned h, float c) { float o; asm("v_fma_mix_f32 %0, %1, %2, 0 op_sel_hi:[1,0,0]" : "=v"(o) : "v"(h), "s"(c)); return o; }
DI float mixhi(unsigned h, float c) { float o; asm("v_fma_mix_f32 %0, %1, %2, 0 op_sel:[1,0,0] op_sel_hi:[1,0,0]" : "=v"(o) : "v"(h), "s"(c)); return o; }
DI float fmul(float a, float b) { float o; asm("v_mul_f32 %0, %1, %2" : "=v"(o) : "v"(a), "v"(b)); return o; }
DI float ffma(float a, float b, float c) { float o; asm("v_fma_f32 %0, %1, %2, %3" : "=v"(o) : "v"(a), "v"(b), "v"(c)); return o; }
DI unsigned la(unsigned lo, unsigned hi, int off, int lim) { return off < lim ? lo + off : hi + (off - lim); }
DI bf16x8 lds128(unsigned a) { return *(LAS bf16x8*)a; }
DI bf16x4 ldstr(unsigned a) { return __builtin_amdgcn_ds_read_tr16_b64_v4i16((LAS bf16x4*)a); }
DI bf16x8 cat(bf16x4 a, bf16x4 b) { return __builtin_shufflevector(a, b, 0, 1, 2, 3, 4, 5, 6, 7); }
typedef __amdgpu_buffer_rsrc_t rsrc_t;
DI rsrc_t mkrs(const void* p, unsigned bytes) { return __builtin_amdgcn_make_buffer_rsrc((void*)p, 0, bytes, 0x00020000); }
DI void st16(rsrc_t ws, unsigned off, u32x4 v, unsigned so = 0) { __builtin_amdgcn_raw_buffer_store_b128(v, ws, off, so, 16); }
DI void st16p(rsrc_t ws, unsigned off, u32x4 v, unsigned so = 0) { __builtin_amdgcn_raw_buffer_store_b128(v, ws, off, so, 0); }
DI void st8(rsrc_t ws, unsigned off, u32x2 v, unsigned so = 0) { __builtin_amdgcn_raw_buffer_store_b64(v, ws, off, so, 16); }
#define LD8(d, ws, off) asm volatile("global_load_dwordx2 %0, %1, %2 sc1" : "=v"(d) : "v"(off), "s"(ws) : "memory")
#define LD4(d, ws, off) asm volatile("global_load_dword %0, %1, %2 sc1" : "=v"(d) : "v"(off), "s"(ws) : "memory")
#define LD16(d, ws, off, imm) asm volatile("global_load_dwordx4 %0, %1, %2 offset:" #imm " sc1" : "=v"(d) : "v"(off), "s"(ws) : "memory")
DI void spin(u32x2 v, char* ws, unsigned off, unsigned t0, unsigned t1) {
#pragma nounroll
  for (int i = 0; i < (1 << 18); i++) {
    if (__builtin_amdgcn_ballot_w64(v[0] != t0 || v[1] != t1) == 0) break;
    __builtin_amdgcn_s_sleep(1);
    LD8(v, ws, off);
    asm volatile("s_waitcnt vmcnt(0)" : "+v"(v) :: "memory");
  }
}

#define TS(i)
#define TW(i)
#define STAMP(i)
#define STAMP_DECL
#define STAMP_OUT()
#define STAMP_ARG

__global__ __launch_bounds__(512, 2) void kf(const u16* __restrict__ x, const u16* __restrict__ Hr, const u16* __restrict__ Hi,
                                             const u16* __restrict__ Wg, const u16* __restrict__ Wu, const u16* __restrict__ Wd,
                                             const u16* __restrict__ gw, const u16* __restrict__ Cr, const u16* __restrict__ Ci,
                                             char* __restrict__ ws, u16* __restrict__ out STAMP_ARG) {
  extern __shared__ __attribute__((aligned(16))) char smem[];
  const int tid = threadIdx.x, lane = tid & 63;
  const int wid = __builtin_amdgcn_readfirstlane(tid >> 6);
  const int b = blockIdx.x, xcd = b & 7, loc = b >> 3;
  const int mi = (xcd >> 2) * 8 + (loc >> 2), kq = xcd & 3, nt = loc & 3, nj = kq * 4 + nt;
  const unsigned sm = (unsigned)(size_t)(LAS char*)smem;
  const int r = lane & 15, q = lane >> 4;
  STAMP_DECL; STAMP(0);
  asm volatile("" :: "s"(x), "s"(Hr), "s"(Hi), "s"(gw), "s"(Wg), "s"(Wu), "s"(Wd), "s"(Cr), "s"(Ci), "s"(ws), "s"(out));
  const rsrc_t wr_ = mkrs(ws, WS_END);
  u32x2 ep;
#define LDEP() asm volatile("s_load_dwordx2 %0, %1, 0\n\ts_waitcnt lgkmcnt(0)" : "=s"(ep) : "s"(ws) : "memory")
  if (__builtin_expect(wid >= 4, 1)) {
    const int w = wid - 4;
    const char* xb = (const char*)x + (size_t)(128 * mi + 8 * w) * 1280;
    const unsigned vx1 = (lane >> 3) * 1280 + (((lane & 7) ^ ((lane >> 4) & 1) ^ (w << 1)) << 4);
    const char* hb = (const char*)(nj >= 8 ? Hi : Hr) + (size_t)(80 * (nj & 7)) * 1280;
    const int hrow = 20 * w + (lane >> 3);
    const unsigned vh0 = hrow * 1280 + (((lane & 7) ^ ((hrow >> 1) & 7)) << 4);
    const unsigned vh1 = (hrow + 8) * 1280 + (((lane & 7) ^ (((hrow + 8) >> 1) & 7)) << 4);
    const char* ph2 = lane < 32 ? hb + vh0 + 16 * 1280 : (const char*)gw + (lane & 7) * 16;
    const bool h2on = lane < (w == 3 ? 40 : 32);
    const int rotk = ((loc >> 2) + ((5 * (loc & 3)) >> 1)) % 10;
    auto issue1 = [&](int t) {
      const unsigned sl = sm + P1B + ((t + 2) & 3) * P1S;
      if (ROTM & 1) { t += rotk; if (t >= 10) t -= 10; }
      const char* xa = xb + t * 128;
#pragma unroll
      for (int j = 0; j < 4; j++) DMA(xa + j * 40960 + vx1, sl + (w + 4 * j) * 1024, 0);
      const char* ha = hb + t * 128;
      const unsigned hl = sl + 16384 + w * 2560;
      DMA(ha + vh0, hl, 0);
      DMA(ha + vh1, hl + 1024, 0);
      if (h2on) DMA(ph2 + t * 128, hl + 2048, 0);
    };
    issue1(0);
    STAMP(1);
    issue1(1); issue1(2); issue1(3);
    const int wp = (((w >> 1) ^ (kq & 1)) << 1) | (w & 1), kh = kq >> 1;
    const char* xb2 = (const char*)x + (size_t)(128 * mi + 8 * wp) * 1280;
    const unsigned vx2 = (lane >> 3) * 1280 + (((lane & 7) ^ (lane >> 4) ^ (4 * (w & 1))) << 4);
    const int fa = (nj & 1) ? 32 : 0, fb = (nj & 1) ? 0 : 64;
    const int sg = ((lane >> 4) & 1) | ((w & 1) << 1);
    const int gc = (lane & 7) ^ (sg << 1);
    const unsigned vw = (lane >> 3) * 3072 + (gc << 4);
    const char* gab = (const char*)Wg + (size_t)(8 * w) * 3072 + (96 * nj + fa) * 2;
    const char* uab = (const char*)Wu + (size_t)(8 * w) * 3072 + (96 * nj + fa) * 2;
    const char* pB = (const char*)((gc & 2) ? Wu : Wg) + (size_t)(8 * w + (lane >> 3)) * 3072 + (96 * nj + fb) * 2 + (((gc & 1) | ((gc >> 1) & 2)) << 4);
    auto issue2 = [&](int t, int slot) {
      if (ROTM & 2) { t += rotk; if (t >= 10) t -= 10; }
      const unsigned sl = sm + slot * SLOT;
      const char* xa = xb2 + t * 128;
#pragma unroll
      for (int j = 0; j < 4; j++) DMA(xa + (j ^ kh) * 40960 + vx2, sl + (w + 4 * j) * 1024, 0);
      const size_t ko = (size_t)t * (64 * 3072);
#pragma unroll
      for (int j = 0; j < 2; j++) {
        DMA(gab + ko + j * (32 * 3072) + vw, sl + 16384 + (w + 4 * j) * 1024, 0);
        DMA(uab + ko + j * (32 * 3072) + vw, sl + 24576 + (w + 4 * j) * 1024, 0);
        DMA(pB + ko + j * (32 * 3072), sl + 32768 + (w + 4 * j) * 1024, 0);
      }
    };
    const char* cb = (const char*)((kq >> 1) ? Ci : Cr) + (size_t)(128 * mi + 8 * wp) * 4096 + (kq & 1) * 2048;
    const unsigned va = (lane >> 3) * 4096 + (((lane & 7) ^ (lane >> 4) ^ (4 * (w & 1))) << 4);
    const char* bb = ws + WS_AB + (size_t)(((kq >> 1) * 64 + 32 * (kq & 1) + (w >> 1)) * 40 + 10 * nt + 5 * (w & 1)) * 1024 + 2048;
    const unsigned vl = lane * 16;
    const int rot3 = 4 * nt + 2 * (loc >> 2);
    auto issue3a = [&](int t, unsigned so) {
      if (ROTM & 4) t = (t + rot3) & 15;
      const char* ca = cb + t * 128;
#pragma unroll
      for (int j = 0; j < 4; j++) DMAP(ca + (j ^ kh) * 131072 + va, sm + so + (w + 4 * j) * 1024, 0, CPOL);
    };
    auto issue3b = [&](int t, unsigned so) {
      if (ROTM & 4) t = (t + rot3) & 15;
      const char* ba = bb + (size_t)t * 81920 + vl;
      const unsigned sb = sm + so + w * 5120 + 2048;
      DMAC(ba, sb, -2048); DMAC(ba, sb, -1024); DMAC(ba, sb, 0); DMAC(ba, sb, 1024); DMAC(ba, sb, 2048);
    };
    auto slot3 = [&](int t) { return ((0x3102u >> (4 * ((t + 2) & 3))) & 3) * SLOT; };
    const char* hh = ws + WS_H + (size_t)((8 * mi + 2 * w) * 48 + 12 * kq) * 1024;
    const char* db = (const char*)Wd + (size_t)(384 * kq + 16 * w) * 1280 + 320 * nt;
    unsigned vd[5];
#pragma unroll
    for (int i = 0; i < 5; i++) {
      const int idx = i * 64 + lane, row = idx / 20, c = idx - row * 20;
      vd[i] = row * 1280 + ((c ^ ((row >> 2) & 2)) << 4);
    }
    const int rot4 = (nt + (loc >> 2) + (nt >> 1)) % 6;
    auto issue4 = [&](int t) {
      const unsigned sl = sm + slot3(t + 16);
      if (ROTM & 8) { t += rot4; if (t >= 6) t -= 6; }
      const char* ha = hh + t * 2048 + vl;
      const unsigned sh = sl + w * 4096;
      DMAC(ha, sh, 0); DMAC(ha, sh, 1024);
      DMAC(ha + 49152 - 2048, sh, 2048); DMAC(ha + 49152 - 2048, sh, 3072);
      const char* da = db + t * (64 * 1280);
      const unsigned sb = sl + 16384 + w * 5120;
#pragma unroll
      for (int i = 0; i < 5; i++) DMA(da + vd[i], sb + i * 1024, 0);
    };
    const unsigned ofa = WS_FA + ((8 * (kq & 1) + ((lane >> 2) & 7)) * 16 + 8 * (kq >> 1) + 2 * nt + (lane >> 5)) * 128 + (lane & 3) * 8;
    const unsigned ofh = WS_FH + (mi * 16 + 4 * kq + ((lane >> 2) & 3)) * 128 + (lane & 3) * 8;
#define NEXT(k) do { TW(k); BAR(); TS(k); } while (0)
    const int pc = PCM == 0 ? 10 + (loc & 7) : PCM;
    u32x2 fv;
    LDEP();
    const unsigned t0 = ep[0] * 1664525u + 1013904223u, t1 = 0x40000000u | ((ep[1] + 0x10000u) & 0x3fff0000u);
    WAITV(21); STAMP(2); BAR();
    if (PWM) { issue2(0, 0); WAITV(24); } else WAITV(14);
    NEXT(0);
#pragma nounroll
    for (int t = 1; t < 7; t++) {
      issue1(t + 3);
      if (PWM && t < 3) WAITV(24); else WAITV(14);
      NEXT(t);
    }
    if (PWM) WAITV(7); else { issue2(0, 0); WAITV(17); }
    NEXT(7);
    issue2(1, 1);
    if (PWM) WAITV(10); else WAITV(20);
    NEXT(8);
    issue2(2, 2); WAITV(20);
    NEXT(9);
    {
      int sl = 2;
#pragma nounroll
      for (int t = 10; t < 16; t++) {
        if (t > 10) issue2(t - 8, sl);
        if (t == pc) { issue3a(0, CE); issue3a(1, CE + 16384); }
        if (t == pc || t == pc + 1) WAITV(18); else WAITV(10);
        NEXT(t);
        sl = sl == 2 ? 0 : sl + 1;
      }
    }
    LD8(fv, ws, ofa);
    issue2(8, 2);
    if (pc == 16) { issue3a(0, CE); issue3a(1, CE + 16384); }
    if (pc == 15 || pc == 16) WAITV(19); else WAITV(11);
    NEXT(16);
    issue2(9, 0);
    if (pc == 17) { issue3a(0, CE); issue3a(1, CE + 16384); }
    if (pc >= 16) WAITV(18); else WAITV(10);
    asm volatile("" : "+v"(fv) :: "memory");
    NEXT(17);
    STAMP(3);
    spin(fv, ws, ofa, t0, t1);
    STAMP(4);
    issue3b(0, SLOT); issue3b(1, SLOT + 20480);
    if (pc == 17) WAITV(18); else WAITV(10);
    NEXT(18);
    issue3a(2, 2 * SLOT); issue3b(2, 2 * SLOT + 16384); WAITV(14);
    NEXT(19);
    issue3a(3, 0); issue3b(3, 16384); WAITV(18);
    NEXT(20);
    WAITV(9);
    NEXT(21);
    issue3a(4, SLOT); issue3b(4, SLOT + 16384);
#pragma nounroll
    for (int t = 22; t < 31; t++) {
      const unsigned so = slot3(t - 17);
      issue3a(t - 17, so); issue3b(t - 17, so + 16384);
      WAITV(18);
      NEXT(t);
    }
    LD8(fv, ws, ofh);
#pragma nounroll
    for (int t = 31; t < 33; t++) {
      const unsigned so = slot3(t - 17);
      issue3a(t - 17, so); issue3b(t - 17, so + 16384);
      WAITV(19);
      NEXT(t);
    }
    asm volatile("s_waitcnt vmcnt(18)" : "+v"(fv) :: "memory");
    STAMP(5);
    spin(fv, ws, ofh, t0, t1);
    STAMP(6);
#pragma nounroll
    for (int t = 33; t < 39; t++) { issue4(t - 33); WAITV(18); NEXT(t); }
    WAITV(9); NEXT(39);
    WAITV(0); NEXT(40);
    if (b == 0 && w == 0) {
      const unsigned oa = WS_FA + (lane >> 2) * 128 + (lane & 3) * 8;
#pragma nounroll
      for (int it = 0; it < (1 << 16); it++) {
        u32x2 f[16];
#pragma unroll
        for (int i = 0; i < 16; i++) LD8(f[i], ws, oa + i * 2048);
        asm volatile("s_waitcnt vmcnt(0)" : "+v"(f[0]), "+v"(f[1]), "+v"(f[2]), "+v"(f[3]), "+v"(f[4]), "+v"(f[5]), "+v"(f[6]), "+v"(f[7]),
                     "+v"(f[8]), "+v"(f[9]), "+v"(f[10]), "+v"(f[11]), "+v"(f[12]), "+v"(f[13]), "+v"(f[14]), "+v"(f[15]) :: "memory");
        unsigned m = 0;
#pragma unroll
        for (int i = 0; i < 16; i++) m |= (f[i][0] ^ t0) | (f[i][1] ^ t1);
        if (__builtin_amdgcn_ballot_w64(m != 0) == 0) break;
        __builtin_amdgcn_s_sleep(2);
      }
      st8(wr_, 0, u32x2{t0, t1}, WS_E);
    }
    STAMP(7); STAMP_OUT();
  } else {
    const int w = wid, wr = w >> 1, wc = w & 1;
    const int z = (r >> 1) & 7;
    if (PRIO) __builtin_amdgcn_s_setprio(PRIO);
    LDEP();
    const unsigned t0 = ep[0] * 1664525u + 1013904223u, t1 = 0x40000000u | ((ep[1] + 0x10000u) & 0x3fff0000u);
    const int grp = mi * 4 + nt;
    const unsigned l16 = lane * 16;
    constexpr int L1 = 2 * P1S, L2 = 2 * SLOT;
    unsigned ax0 = sm + P1B + (8 * (r >> 2) + (r & 3)) * 128 + ((q ^ z) << 4) + 4096 * w, ax1 = ax0 ^ 64;
    unsigned ah0 = sm + P1B + 16384 + r * 128 + ((q ^ z) << 4), ah1 = ah0 ^ 64, ag0 = sm + P1B + 16384 + 10240 + (q << 4);
    unsigned ax0h = ax0 + L1, ax1h = ax1 + L1, ah0h = ah0 + L1, ah1h = ah1 + L1, ag0h = ag0 + L1;
    OPQ(ax0); OPQ(ax1); OPQ(ah0); OPQ(ah1); OPQ(ag0); OPQ(ax0h); OPQ(ax1h); OPQ(ah0h); OPQ(ah1h); OPQ(ag0h);
    f32x4 c1[2][6];
    bf16x8 p1a[2][2], p1b[2][6];
    auto rd1 = [&](int T, int h, int k) INL {
      const int so = ((T + 2) & 3) * P1S;
      if (k == 0 || k == 2) p1a[h][k >> 1] = lds128(la(h ? ax1 : ax0, h ? ax1h : ax0h, so + (k >> 1) * 512, L1));
      else if (k < 7) { const int j = k == 1 ? 0 : k - 2; p1b[h][j] = lds128(la(h ? ah1 : ah0, h ? ah1h : ah0h, so + j * 2048, L1)); }
      else if (k == 7) p1b[h][5] = lds128(la(ag0, ag0h, so + h * 64, L1));
      SB0();
    };
    unsigned bx0 = sm + (64 * wr + r) * 128 + ((q ^ z) << 4), bx1 = bx0 ^ 64;
    const int qq = r >> 2, sg = ((qq >> 1) & 1) | ((q & 1) << 1);
    unsigned tA0 = sm + (8 * q + qq) * 128 + 8 * (lane & 3) + (((2 * wc) ^ sg) << 5) + 16384, tA1 = tA0 ^ 32;
    unsigned bx0h = bx0 + L2, bx1h = bx1 + L2, tA0h = tA0 + L2, tA1h = tA1 + L2;
    OPQ(bx0); OPQ(bx1); OPQ(tA0); OPQ(tA1); OPQ(bx0h); OPQ(bx1h); OPQ(tA0h); OPQ(tA1h);
    f32x4 cg[12], cu[12];
    bf16x8 xx[2][4]; bf16x4 wl[2][6], wh[2][6];
    auto rd2 = [&](int t, int h, int k) INL {
      const int so = (t % 3) * SLOT;
      if (k >= 2 && k < 6) xx[h][k - 2] = lds128(la(h ? bx1 : bx0, h ? bx1h : bx0h, so + (k - 2) * 2048, L2));
      else if (k < 16) {
        const int kk = k < 2 ? k : k - 4, f = kk >> 1;
        const bool s1 = f == 2 || f == 3 || f == 5;
        const int off = so + h * 4096 + (kk & 1) * 512 + (f >= 4 ? 16384 : (f & 1) * 8192);
        const bf16x4 v = ldstr(la(s1 ? tA1 : tA0, s1 ? tA1h : tA0h, off, L2));
        if (kk & 1) wh[h][f] = v; else wl[h][f] = v;
      }
      SB0();
    };
    unsigned ab0 = sm + wc * 5120 + lane * 16 + 16384, ab0h = ab0 + L2;
    OPQ(ab0); OPQ(ab0h);
    f32x4 acc[4][5];
    bf16x8 fa[2][4], fb[2][5]; bf16x4 dl[2][5], dh_[2][5];
    auto sa3 = [&](int t) INL { return t < 2 ? CE + 16384 * t : (int)((0x3102u >> (4 * ((t + 2) & 3))) & 3) * SLOT; };
    auto sb3 = [&](int t) INL { return t < 2 ? SLOT - 16384 + 20480 * t : sa3(t); };
    auto rd3 = [&](int t, int h, int k) INL {
      if (k >= 1 && k < 5) fa[h][k - 1] = lds128(la(h ? bx1 : bx0, h ? bx1h : bx0h, sa3(t) + (k - 1) * 2048, L2));
      else if (k < 9) { const int j = k == 0 ? 0 : k - 4; fb[h][j] = lds128(la(ab0, ab0h, sb3(t) + h * 10240 + j * 1024, L2)); }
      SB0();
    };
    BAR();
    STAMP(1);
    UNR for (int k = 0; k < 8; k++) rd1(0, 0, k);
    LGKM0(); SB0();
    SF(t, 10)
      UNR for (int k = 0; k < 12; k++) {
        if (t == 0) MFMAZ(p1a[0][k & 1], p1b[0][k >> 1], c1[k & 1][k >> 1]); else MFMAT(p1a[0][k & 1], p1b[0][k >> 1], c1[k & 1][k >> 1]);
        rd1(t, 1, k);
        SB0();
      }
      LGKM0(); BAR(); SB0();
      UNR for (int k = 0; k < 12; k++) {
        MFMAT(p1a[1][k & 1], p1b[1][k >> 1], c1[k & 1][k >> 1]);
        if (t < 9) rd1(t + 1, 0, k); else rd2(0, 0, k);
        SB0();
      }
      if (t == 9) { UNR for (int k = 12; k < 16; k++) rd2(0, 0, k); } else LGKM0();
      SB0();
    SE
    STAMP(2);
    float gv[4], iog[4];
    u32x4 abp[5], glp;
    u32x2 gp[12];
    const unsigned dab = WS_AB + ((((nj >> 3) * 64 + 4 * mi + w) * 40) + 5 * (nj & 7)) * 1024;
    const unsigned go = WS_G + b * 512 + (64 * wr + r) * 4;
    SF(t, 10)
      SF(h, 2)
        UNR for (int k = 0; k < 24; k++) {
          const bool lastH = t == 9 && h == 1;
          const int f = lastH ? (k < 12 ? 2 * (k >> 2) : 2 * ((k - 12) >> 2) + 1) : k >> 2;
          const int i = k & 3, n = 4 * (f >> 1) + i;
          const bf16x8 wf = cat(wl[h][f], wh[h][f]);
          if (t == 0 && h == 0) { if (f & 1) MFMAZ(wf, xx[h][i], cu[n]); else MFMAZ(wf, xx[h][i], cg[n]); }
          else { if (f & 1) MFMAT(wf, xx[h][i], cu[n]); else MFMAT(wf, xx[h][i], cg[n]); }
          if (h == 0) rd2(t, 1, k); else if (t < 9) rd2(t + 1, 0, k); else rd3(0, 0, k);
          if (t == 0 && h == 0 && k >= 4) { const int e = k - 4, j = e >> 2, c = e & 3; abp[j][c] = pk2(c1[c >> 1][j][2 * (c & 1)], c1[c >> 1][j][2 * (c & 1) + 1]); }
          if (t == 0 && h == 1) {
            if (k < 4) glp[k] = __float_as_uint((r & 1) ? c1[1][5][k] : c1[0][5][k]);
            if (!(XP & 2) && k >= 16 && k < 21) st16(wr_, l16 + ((k - 16) & 3) * 1024, abp[k - 16], dab + ((k - 16) >> 2) * 4096);
            if (k == 21) st16p(wr_, WS_G + (8 * q + 4 * (r & 1)) * 4, glp, b * 512 + w * 128);
          }
          if (t == PUB1 + 2 && k >= 16) {
            const int i2 = 2 * h + ((k - 16) >> 2), e = (k - 16) & 3;
            if (e == 0) gv[i2] = gv[i2] * 1.44269504088896341f;
            if (e == 1) gv[i2] = __builtin_amdgcn_exp2f(gv[i2]);
            if (e == 2) iog[i2] = fminf(gv[i2] + 1.f, 3.0e38f);
            if (e == 3) gv[i2] = __builtin_amdgcn_rcpf(iog[i2]);
          }
          if (t == PUB1 + 3 && h == 0 && k >= 20) gv[k - 20] = kq >= 2 ? gv[k - 20] - 1.f : 1.f - gv[k - 20];
          if (lastH && k >= 10 && k < 22) gp[k - 10] = u32x2{pkh(cg[k - 10][0], cg[k - 10][1]), pkh(cg[k - 10][2], cg[k - 10][3])};
          SB0();
        }
        if (h == 0) {
          if (t == PUB1) { WAITV(0); st8(wr_, 0, u32x2{t0, t1}, WS_FA + (mi * 16 + nj) * 128 + w * 8); }
          if (t == PUB1 + 1) asm volatile("s_waitcnt vmcnt(0)" : "+v"(gv[0]), "+v"(gv[1]), "+v"(gv[2]), "+v"(gv[3]) :: "memory");
          LGKM0(); BAR();
          if (t == PUB1) {
            UNR for (int i = 0; i < 4; i++) asm volatile("global_load_dword %0, %1, %2" : "=v"(gv[i]) : "v"(go + 64 * (i ^ kq)), "s"(ws) : "memory");
          }
        } else LGKM0();
        SB0();
      SE
    SE
    STAMP(3);
    unsigned ah4 = sm + wr * 8192 + lane * 16;
    unsigned tde = sm + 16384 + (8 * q + (r >> 2)) * 320 + 8 * (lane & 3) + 160 * wc + ((q & 1) ? (wc ? -32 : 32) : 0);
    unsigned tdo = sm + 16384 + (8 * q + (r >> 2)) * 320 + 8 * (lane & 3) + 160 * wc + ((q & 1) ? (wc ? 32 : -32) : 0);
    unsigned ah4h, tdeh, tdoh;
    auto rd4 = [&](int t, int h, int k) INL {
      const int so = sa3(t + 16);
      if (k >= 2 && k < 6) fa[h][k - 2] = lds128(la(ah4, ah4h, so + h * 1024 + (k - 2) * 2048, L2));
      else if (k < 14) {
        const int kk = k < 2 ? k : k - 4, j = kk >> 1;
        const bf16x4 v = ldstr(la((j & 1) ? tdo : tde, (j & 1) ? tdoh : tdeh, so + h * 10240 + 32 * j + (kk & 1) * 1280, L2));
        if (kk & 1) dh_[h][j] = v; else dl[h][j] = v;
      }
      SB0();
    };
    {
      const int fa16 = (nj & 1) ? 2 : 0, fb16 = (nj & 1) ? 0 : 4;
      const unsigned dh = WS_H + ((8 * mi + 4 * wr) * 48 + 3 * nj) * 1024, dhv = (q & 1) * 49152 + ((q >> 1) * 16 + r) * 16;
      const float nl2e = -1.44269504088896341f;
      float ta[4], tb[4]; u32x2 e0, e1; unsigned s0[2], s1[2];
      auto mix = [&](int n, int c) INL { return (c & 1) ? mixhi(gp[n][c >> 1], nl2e) : mixlo(gp[n][c >> 1], nl2e); };
      SF(t, 16)
        UNR for (int k = 0; k < 20; k++) {
          if (t == 0) MFMAZ(fb[0][k >> 2], fa[0][k & 3], acc[k & 3][k >> 2]); else MFMAT(fb[0][k >> 2], fa[0][k & 3], acc[k & 3][k >> 2]);
          rd3(t, 1, k);
          const int n = 2 * t;
          if (t >= 1 && t < 7) {
            if (k == 0) e1[1] = pk2(tb[2], tb[3]);
            if (k == 4) { auto s = __builtin_amdgcn_permlane16_swap(e0[0], e1[0], false, false); s0[0] = s[0]; s0[1] = s[1]; }
            if (k == 5) { auto s = __builtin_amdgcn_permlane16_swap(e0[1], e1[1], false, false); s1[0] = s[0]; s1[1] = s[1]; }
            if (k == 6) {
              const int c = (t - 1) >> 1, blk = c < 2 ? fa16 + 2 * wc + c : fb16 + wc;
              st16(wr_, dhv, u32x4{s0[0], s1[0], s0[1], s1[1]}, dh + (blk >> 1) * 1024 + (blk & 1) * 512 + ((t - 1) & 1) * 98304);
            }
          }
          if (t < 6) {
            if (k < 4) ta[k] = mix(n, k);
            if (k == 7 || k == 8) tb[k - 7] = mix(n + 1, k - 7);
            if (k >= 9 && k < 13) ta[k - 9] = __builtin_amdgcn_exp2f(ta[k - 9]);
            if (k == 13 || k == 14) { const int c = 2 * (k - 13); ta[c] = ffma(ta[c], iog[n & 3], iog[n & 3]); ta[c + 1] = ffma(ta[c + 1], iog[n & 3], iog[n & 3]); }
            if (k >= 15 && k < 19) ta[k - 15] = __builtin_amdgcn_rcpf(ta[k - 15]);
            if (k == 19) { ta[0] = fmul(ta[0], cu[n][0]); ta[1] = fmul(ta[1], cu[n][1]); }
          }
          if (t == 12 && k >= 12 && k < 15) {
            if (k == 12) { OPQ(ah4); ah4h = ah4 + L2; OPQ(ah4h); }
            if (k == 13) { OPQ(tde); tdeh = tde + L2; OPQ(tdeh); }
            if (k == 14) { OPQ(tdo); tdoh = tdo + L2; OPQ(tdoh); }
          }
          SB0();
        }
        if (t == PUB2) { WAITV(0); st8(wr_, 0, u32x2{t0, t1}, WS_FH + (mi * 16 + nj) * 128 + w * 8); }
        LGKM0(); BAR(); SB0();
        UNR for (int k = 0; k < 20; k++) {
          MFMAT(fb[1][k >> 2], fa[1][k & 3], acc[k & 3][k >> 2]);
          const int n = 2 * t + 1;
          if (t < 6) {
            const int kr = k < 6 ? k : k == 10 ? 6 : k == 11 ? 7 : k == 14 ? 8 : 99;
            rd3(t + 1, 0, kr);
            if (k < 2) ta[2 + k] = fmul(ta[2 + k], cu[n - 1][2 + k]);
            if (k == 2) e0[0] = pk2(ta[0], ta[1]);
            if (k == 3) e0[1] = pk2(ta[2], ta[3]);
            if (k == 4 || k == 5) tb[k - 2] = mix(n, k - 2);
            if (k >= 6 && k < 10) tb[k - 6] = __builtin_amdgcn_exp2f(tb[k - 6]);
            if (k == 10 || k == 11) tb[k - 10] = ffma(tb[k - 10], iog[n & 3], iog[n & 3]);
            if (k == 12 || k == 13) tb[k - 12] = __builtin_amdgcn_rcpf(tb[k - 12]);
            if (k == 14) tb[2] = ffma(tb[2], iog[n & 3], iog[n & 3]);
            if (k == 15) { tb[3] = ffma(tb[3], iog[n & 3], iog[n & 3]); tb[0] = fmul(tb[0], cu[n][0]); }
            if (k == 16 || k == 17) tb[k - 14] = __builtin_amdgcn_rcpf(tb[k - 14]);
            if (k == 18) { tb[1] = fmul(tb[1], cu[n][1]); tb[2] = fmul(tb[2], cu[n][2]); }
            if (k == 19) { tb[3] = fmul(tb[3], cu[n][3]); e1[0] = pk2(tb[0], tb[1]); }
          } else if (t < 15) rd3(t + 1, 0, k); else rd4(0, 0, k);
          SB0();
        }
        if (t >= 6) LGKM0();
        SB0();
      SE
      STAMP(5);
      UNR for (int j = 0; j < 5; j++) {
        UNR for (int i = 0; i < 4; i++) acc[i][j] *= gv[i];
        SB0();
      }
    }
    SF(t, 5)
      SF(h, 2)
        UNR for (int k = 0; k < 20; k++) {
          MFMAT(cat(dl[h][k >> 2], dh_[h][k >> 2]), fa[h][k & 3], acc[k & 3][k >> 2]);
          if (h == 0) rd4(t, 1, k); else rd4(t + 1, 0, k);
          SB0();
        }
        LGKM0();
        if (h == 0) BAR();
        SB0();
      SE
    SE
    unsigned pv;
    const unsigned pi = WS_P + ((grp * 4 + kq) * 16 + w) * 3072;
    const unsigned ofp = pi + (1 + (lane & 3) % 3) * 12288 + 3068;
    {
      u32x4 o0, o1, o2;
      const float tf = __uint_as_float(t1);
      UNR for (int k = 0; k < 40; k++) {
        const int h = k < 15 ? 0 : k < 30 ? 1 : (k - 30) / 5;
        const int i = k < 15 ? 1 + k % 3 : k < 30 ? 1 + (k - 15) / 5 : 0;
        const int j = k < 15 ? k / 3 : k % 5;
        MFMAT(cat(dl[h][j], dh_[h][j]), fa[h][i], acc[i][j]);
        if (k < 14) rd4(5, 1, k);
        if (k == 14) LGKM0();
        if (POLL && k == POLLK) LD4(pv, ws, ofp);
        if (k >= 23 && k < 38) {
          const int i1 = 1 + (k - 23) / 5, j1 = (k - 23) % 5;
          const f32x4 v = acc[i1][j1];
          const unsigned po = WS_P + (((grp * 4 + (kq ^ i1)) * 4 + i1) * 4 + w) * 3072;
          if (j1 == 0) { o0[0] = pk2(v[2], v[3]); o0[3] = pk2(v[0], tf); }
          if (j1 == 1) { o0[1] = pk2(v[0], v[1]); o0[2] = pk2(v[2], v[3]); st16(wr_, l16, o0, po); }
          if (j1 == 2) { o1[0] = pk2(v[0], v[1]); o1[1] = pk2(v[2], v[3]); }
          if (j1 == 3) { o1[2] = pk2(v[0], v[1]); o1[3] = pk2(acc[i1][0][1], tf); st16(wr_, l16 + 1024, o1, po); }
          if (j1 == 4) { o2[0] = pk2(acc[i1][3][2], acc[i1][3][3]); o2[1] = pk2(v[0], v[1]); o2[2] = pk2(v[2], v[3]); o2[3] = t0; st16(wr_, l16 + 2048, o2, po); }
        }
        SB0();
      }
    }
    STAMP(7);
    {
      u32x4 p0[3], p1[3], p2[3];
      if (POLL) {
        asm volatile("s_waitcnt vmcnt(9)" : "+v"(pv) :: "memory");
#pragma nounroll
        for (int it = 0; it < (1 << 16); it++) {
          if (__builtin_amdgcn_ballot_w64(pv != t0) == 0) break;
          if (POLL > 1) __builtin_amdgcn_s_sleep(POLL);
          LD4(pv, ws, ofp);
          asm volatile("s_waitcnt vmcnt(0)" : "+v"(pv) :: "memory");
        }
        STAMP(9);
      }
#pragma nounroll
      for (int it = 0; it < (1 << 16); it++) {
#pragma unroll
        for (int s = 0; s < 3; s++) {
          const unsigned ps = pi + l16 + (s + 1) * 12288;
          LD16(p0[s], ws, ps, 0); LD16(p1[s], ws, ps, 1024); LD16(p2[s], ws, ps, 2048);
        }
        asm volatile("s_waitcnt vmcnt(0)" : "+v"(p0[0]), "+v"(p0[1]), "+v"(p0[2]), "+v"(p1[0]), "+v"(p1[1]), "+v"(p1[2]), "+v"(p2[0]), "+v"(p2[1]), "+v"(p2[2]) :: "memory");
        unsigned m = 0, m2 = 0;
#pragma unroll
        for (int s = 0; s < 3; s++) { m |= (p0[s][3] ^ t1) | (p1[s][3] ^ t1); m2 |= p2[s][3] ^ t0; }
        if (it == 0) STAMP(8);
        if (__builtin_amdgcn_ballot_w64(((m >> 16) | m2) != 0) == 0) break;
        __builtin_amdgcn_s_sleep(2);
      }
      STAMP(10);
      f32x4 y[5];
#pragma unroll
      for (int j = 0; j < 5; j++) y[j] = acc[0][j];
#pragma unroll
      for (int s = 0; s < 3; s++) {
        y[0][0] += bflo(p0[s][3]); y[0][1] += bflo(p1[s][3]); y[0][2] += bflo(p0[s][0]); y[0][3] += bfhi(p0[s][0]);
        y[1][0] += bflo(p0[s][1]); y[1][1] += bfhi(p0[s][1]); y[1][2] += bflo(p0[s][2]); y[1][3] += bfhi(p0[s][2]);
        y[2][0] += bflo(p1[s][0]); y[2][1] += bfhi(p1[s][0]); y[2][2] += bflo(p1[s][1]); y[2][3] += bfhi(p1[s][1]);
        y[3][0] += bflo(p1[s][2]); y[3][1] += bfhi(p1[s][2]); y[3][2] += bflo(p2[s][0]); y[3][3] += bfhi(p2[s][0]);
        y[4][0] += bflo(p2[s][1]); y[4][1] += bfhi(p2[s][1]); y[4][2] += bflo(p2[s][2]); y[4][3] += bfhi(p2[s][2]);
      }
      char* dst = (char*)out + (size_t)(128 * mi + 64 * wr + 16 * kq + r) * 1280 + (160 * nt + 80 * wc) * 2 + q * 8;
#pragma unroll
      for (int j = 0; j < 5; j++) *(GAS u32x2*)(dst + j * 32) = u32x2{pk2(y[j][0], y[j][1]), pk2(y[j][2], y[j][3])};
      STAMP(11); STAMP_OUT();
    }
  }
}

extern "C" void kernel_launch(void* const* d_in, const int* in_sizes, int n_in, void* d_out, int out_size, void* d_ws, size_t ws_size,
                              hipStream_t stream) {
  (void)hipFuncSetAttribute((const void*)kf, hipFuncAttributeMaxDynamicSharedMemorySize, LDS_BYTES);
  kf<<<256, 512, LDS_BYTES, stream>>>((const u16*)d_in[0], (const u16*)d_in[3], (const u16*)d_in[4], (const u16*)d_in[6], (const u16*)d_in[7],
                                      (const u16*)d_in[8], (const u16*)d_in[9], (const u16*)d_in[1], (const u16*)d_in[2], (char*)d_ws, (u16*)d_out);
}
```

```cpp
#include <hip/hip_runtime.h>
#include <stdint.h>
typedef __attribute__((ext_vector_type(8))) short bf16x8;
typedef __attribute__((ext_vector_type(4))) short bf16x4;
typedef __attribute__((ext_vector_type(4))) float f32x4;
typedef __attribute__((ext_vector_type(2))) float f32x2;
typedef __attribute__((ext_vector_type(2))) __bf16 bfx2;
typedef __attribute__((ext_vector_type(4))) unsigned u32x4;
typedef __attribute__((ext_vector_type(2))) unsigned u32x2;
typedef unsigned short u16;
#define GAS __attribute__((address_space(1)))
#define LAS __attribute__((address_space(3)))
#define DI __device__ __forceinline__

constexpr int SLOT = 40960;
constexpr int LDS_BYTES = 4 * SLOT;
constexpr int P1B = 52736, P1S = 26752;
constexpr int CE = 3 * SLOT;
constexpr unsigned WS_E  = 0;
constexpr unsigned WS_FA = 1u << 16;
constexpr unsigned WS_FH = 2u << 16;
constexpr unsigned WS_G  = 8u << 16;
constexpr unsigned WS_AB = 1u << 20;
constexpr unsigned WS_H  = 8u << 20;
constexpr unsigned WS_P  = 20u << 20;
constexpr unsigned WS_END = 40u << 20;

#define CPOL 0
#define ROTM 15
#define PWM 1
#define PCM 0
#define PUB1 2
#define PUB2 8
#define XP 0
#define PRIO 0
#define POLL 1
#define R3 0
#define CWN 0
#define CW 0
#define POLLK 22
#define TLBW 0
#define PRE 0
#define PRET 7
#define CPF 0
#define DMAP(g, l, off, pol) __builtin_amdgcn_global_load_lds((const GAS void*)(g), (LAS void*)(l), 16, off, pol)
#define DMA(g, l, off) DMAP(g, l, off, 0)
#define XSC 16
#define DMAC(g, l, off) DMAP(g, l, off, XSC)
#define WAITV(n) asm volatile("s_waitcnt vmcnt(" #n ")" ::: "memory")
#define BAR() do { asm volatile("" ::: "memory"); __builtin_amdgcn_s_barrier(); asm volatile("" ::: "memory"); } while (0)
#define LGKM0() __builtin_amdgcn_s_waitcnt(0xC07F)
#define MFMAT(a, b, c) do { asm volatile("v_mfma_f32_16x16x32_bf16 %0, %1, %2, %0" : "+v"(c) : "v"(a), "v"(b)); __builtin_amdgcn_sched_barrier(0); } while (0)
#define MFMAZ(a, b, c) do { asm volatile("v_mfma_f32_16x16x32_bf16 %0, %1, %2, 0" : "=&v"(c) : "v"(a), "v"(b)); __builtin_amdgcn_sched_barrier(0); } while (0)
#define OPQ(v) asm volatile("" : "+v"(v))
#define UNR _Pragma("unroll")
#define INL __attribute__((always_inline))
#define SB0() __builtin_amdgcn_sched_barrier(0)
template <int N> struct IC { static constexpr int v = N; };
template <int I, int N, class F> DI void sfor(F&& f) { if constexpr (I < N) { f(IC<I>{}); sfor<I + 1, N>(f); } }
#define SF(var, n) sfor<0, n>([&](auto var##_c) INL { constexpr int var = decltype(var##_c)::v;
#define SE });

DI unsigned pk2(float a, float b) { f32x2 v = {a, b}; return __builtin_bit_cast(unsigned, __builtin_convertvector(v, bfx2)); }
DI float bflo(unsigned v) { return __uint_as_float(v << 16); }
DI float bfhi(unsigned v) { return __uint_as_float(v & 0xffff0000u); }
DI unsigned pkh(float a, float b) { return __builtin_bit_cast(unsigned, __builtin_amdgcn_cvt_pkrtz(a, b)); }
DI float mixlo(unsigned h, float c) { float o; asm("v_fma_mix_f32 %0, %1, %2, 0 op_sel_hi:[1,0,0]" : "=v"(o) : "v"(h), "s"(c)); return o; }
DI float mixhi(unsigned h, float c) { float o; asm("v_fma_mix_f32 %0, %1, %2, 0 op_sel:[1,0,0] op_sel_hi:[1,0,0]" : "=v"(o) : "v"(h), "s"(c)); return o; }
DI float fmul(float a, float b) { float o; asm("v_mul_f32 %0, %1, %2" : "=v"(o) : "v"(a), "v"(b)); return o; }
DI float ffma(float a, float b, float c) { float o; asm("v_fma_f32 %0, %1, %2, %3" : "=v"(o) : "v"(a), "v"(b), "v"(c)); return o; }
DI unsigned la(unsigned lo, unsigned hi, int off, int lim) { return off < lim ? lo + off : hi + (off - lim); }
DI bf16x8 lds128(unsigned a) { return *(LAS bf16x8*)a; }
DI bf16x4 ldstr(unsigned a) { return __builtin_amdgcn_ds_read_tr16_b64_v4i16((LAS bf16x4*)a); }
DI bf16x8 cat(bf16x4 a, bf16x4 b) { return __builtin_shufflevector(a, b, 0, 1, 2, 3, 4, 5, 6, 7); }
typedef __amdgpu_buffer_rsrc_t rsrc_t;
DI rsrc_t mkrs(const void* p, unsigned bytes) { return __builtin_amdgcn_make_buffer_rsrc((void*)p, 0, bytes, 0x00020000); }
DI void st16(rsrc_t ws, unsigned off, u32x4 v, unsigned so = 0) { __builtin_amdgcn_raw_buffer_store_b128(v, ws, off, so, 16); }
DI void st16p(rsrc_t ws, unsigned off, u32x4 v, unsigned so = 0) { __builtin_amdgcn_raw_buffer_store_b128(v, ws, off, so, 0); }
DI void st8(rsrc_t ws, unsigned off, u32x2 v, unsigned so = 0) { __builtin_amdgcn_raw_buffer_store_b64(v, ws, off, so, 16); }
#define LD8(d, ws, off) asm volatile("global_load_dwordx2 %0, %1, %2 sc1" : "=v"(d) : "v"(off), "s"(ws) : "memory")
#define LD4(d, ws, off) asm volatile("global_load_dword %0, %1, %2 sc1" : "=v"(d) : "v"(off), "s"(ws) : "memory")
#define LD16(d, ws, off, imm) asm volatile("global_load_dwordx4 %0, %1, %2 offset:" #imm " sc1" : "=v"(d) : "v"(off), "s"(ws) : "memory")
DI void spin(u32x2 v, char* ws, unsigned off, unsigned t0, unsigned t1) {
#pragma nounroll
  for (int i = 0; i < (1 << 18); i++) {
    if (__builtin_amdgcn_ballot_w64(v[0] != t0 || v[1] != t1) == 0) break;
    __builtin_amdgcn_s_sleep(1);
    LD8(v, ws, off);
    asm volatile("s_waitcnt vmcnt(0)" : "+v"(v) :: "memory");
  }
}

#define TS(i)
#define TW(i)
#define STAMP(i)
#define STAMP_DECL
#define STAMP_OUT()
#define STAMP_ARG

__global__ __launch_bounds__(512, 2) void kf(const u16* __restrict__ x, const u16* __restrict__ Hr, const u16* __restrict__ Hi,
                                             const u16* __restrict__ Wg, const u16* __restrict__ Wu, const u16* __restrict__ Wd,
                                             const u16* __restrict__ gw, const u16* __restrict__ Cr, const u16* __restrict__ Ci,
                                             char* __restrict__ ws, u16* __restrict__ out STAMP_ARG) {
  extern __shared__ __attribute__((aligned(16))) char smem[];
  const int tid = threadIdx.x, lane = tid & 63;
  const int wid = __builtin_amdgcn_readfirstlane(tid >> 6);
  const int b = blockIdx.x, xcd = b & 7, loc = b >> 3;
  const int mi = (xcd >> 2) * 8 + (loc >> 2), kq = xcd & 3, nt = loc & 3, nj = kq * 4 + nt;
  const unsigned sm = (unsigned)(size_t)(LAS char*)smem;
  const int r = lane & 15, q = lane >> 4;
  STAMP_DECL; STAMP(0);
  asm volatile("" :: "s"(x), "s"(Hr), "s"(Hi), "s"(gw), "s"(Wg), "s"(Wu), "s"(Wd), "s"(Cr), "s"(Ci), "s"(ws), "s"(out));
  const rsrc_t wr_ = mkrs(ws, WS_END);
  u32x2 ep;
#define LDEP() asm volatile("s_load_dwordx2 %0, %1, 0\n\ts_waitcnt lgkmcnt(0)" : "=s"(ep) : "s"(ws) : "memory")
  if (__builtin_expect(wid >= 4, 1)) {
    const int w = wid - 4;
    const char* xb = (const char*)x + (size_t)(128 * mi + 8 * w) * 1280;
    const unsigned vx1 = (lane >> 3) * 1280 + (((lane & 7) ^ ((lane >> 4) & 1) ^ (w << 1)) << 4);
    const char* hb = (const char*)(nj >= 8 ? Hi : Hr) + (size_t)(80 * (nj & 7)) * 1280;
    const int hrow = 20 * w + (lane >> 3);
    const unsigned vh0 = hrow * 1280 + (((lane & 7) ^ ((hrow >> 1) & 7)) << 4);
    const unsigned vh1 = (hrow + 8) * 1280 + (((lane & 7) ^ (((hrow + 8) >> 1) & 7)) << 4);
    const char* ph2 = lane < 32 ? hb + vh0 + 16 * 1280 : (const char*)gw + (lane & 7) * 16;
    const bool h2on = lane < (w == 3 ? 40 : 32);
    const int rotk = ((loc >> 2) + ((5 * (loc & 3)) >> 1)) % 10;
    auto issue1 = [&](int t) {
      const unsigned sl = sm + P1B + ((t + 2) & 3) * P1S;
      if (ROTM & 1) { t += rotk; if (t >= 10) t -= 10; }
      const char* xa = xb + t * 128;
#pragma unroll
      for (int j = 0; j < 4; j++) DMA(xa + j * 40960 + vx1, sl + (w + 4 * j) * 1024, 0);
      const char* ha = hb + t * 128;
      const unsigned hl = sl + 16384 + w * 2560;
      DMA(ha + vh0, hl, 0);
      DMA(ha + vh1, hl + 1024, 0);
      if (h2on) DMA(ph2 + t * 128, hl + 2048, 0);
    };
    issue1(0);
    STAMP(1);
    issue1(1); issue1(2); issue1(3);
    const int wp = (((w >> 1) ^ (kq & 1)) << 1) | (w & 1), kh = kq >> 1;
    const char* xb2 = (const char*)x + (size_t)(128 * mi + 8 * wp) * 1280;
    const unsigned vx2 = (lane >> 3) * 1280 + (((lane & 7) ^ (lane >> 4) ^ (4 * (w & 1))) << 4);
    const int fa = (nj & 1) ? 32 : 0, fb = (nj & 1) ? 0 : 64;
    const int sg = ((lane >> 4) & 1) | ((w & 1) << 1);
    const int gc = (lane & 7) ^ (sg << 1);
    const unsigned vw = (lane >> 3) * 3072 + (gc << 4);
    const char* gab = (const char*)Wg + (size_t)(8 * w) * 3072 + (96 * nj + fa) * 2;
    const char* uab = (const char*)Wu + (size_t)(8 * w) * 3072 + (96 * nj + fa) * 2;
    const char* pB = (const char*)((gc & 2) ? Wu : Wg) + (size_t)(8 * w + (lane >> 3)) * 3072 + (96 * nj + fb) * 2 + (((gc & 1) | ((gc >> 1) & 2)) << 4);
    auto issue2 = [&](int t, int slot) {
      if (ROTM & 2) { t += rotk; if (t >= 10) t -= 10; }
      const unsigned sl = sm + slot * SLOT;
      const char* xa = xb2 + t * 128;
#pragma unroll
      for (int j = 0; j < 4; j++) DMA(xa + (j ^ kh) * 40960 + vx2, sl + (w + 4 * j) * 1024, 0);
      const size_t ko = (size_t)t * (64 * 3072);
#pragma unroll
      for (int j = 0; j < 2; j++) {
        DMA(gab + ko + j * (32 * 3072) + vw, sl + 16384 + (w + 4 * j) * 1024, 0);
        DMA(uab + ko + j * (32 * 3072) + vw, sl + 24576 + (w + 4 * j) * 1024, 0);
        DMA(pB + ko + j * (32 * 3072), sl + 32768 + (w + 4 * j) * 1024, 0);
      }
    };
    const char* cb = (const char*)((kq >> 1) ? Ci : Cr) + (size_t)(128 * mi + 8 * wp) * 4096 + (kq & 1) * 2048;
    const unsigned va = (lane >> 3) * 4096 + (((lane & 7) ^ (lane >> 4) ^ (4 * (w & 1))) << 4);
    const char* bb = ws + WS_AB + (size_t)(((kq >> 1) * 64 + 32 * (kq & 1) + (w >> 1)) * 40 + 10 * nt + 5 * (w & 1)) * 1024 + 2048;
    const unsigned vl = lane * 16;
    const int rot3 = (R3 == 0 ? 4 * nt : R3 == 1 ? 8 * (nt >> 1) : R3 == 2 ? 8 * (nt & 1) + 2 * (nt >> 1) : R3 == 3 ? nt : 0) + 2 * (loc >> 2);
    auto issue3a = [&](int t, unsigned so) {
      if (ROTM & 4) t = (t + rot3) & 15;
      const char* ca = cb + t * 128;
#pragma unroll
      for (int j = 0; j < 4; j++) DMAP(ca + (j ^ kh) * 131072 + va, sm + so + (w + 4 * j) * 1024, 0, CPOL);
    };
    auto issue3b = [&](int t, unsigned so) {
      if (ROTM & 4) t = (t + rot3) & 15;
      const char* ba = bb + (size_t)t * 81920 + vl;
      const unsigned sb = sm + so + w * 5120 + 2048;
      DMAC(ba, sb, -2048); DMAC(ba, sb, -1024); DMAC(ba, sb, 0); DMAC(ba, sb, 1024); DMAC(ba, sb, 2048);
    };
    auto slot3 = [&](int t) { return ((0x3102u >> (4 * ((t + 2) & 3))) & 3) * SLOT; };
    const char* hh = ws + WS_H + (size_t)((8 * mi + 2 * w) * 48 + 12 * kq) * 1024;
    const char* db = (const char*)Wd + (size_t)(384 * kq + 16 * w) * 1280 + 320 * nt;
    unsigned vd[5];
#pragma unroll
    for (int i = 0; i < 5; i++) {
      const int idx = i * 64 + lane, row = idx / 20, c = idx - row * 20;
      vd[i] = row * 1280 + ((c ^ ((row >> 2) & 2)) << 4);
    }
    const int rot4 = (nt + (loc >> 2) + (nt >> 1)) % 6;
    auto issue4 = [&](int t) {
      const unsigned sl = sm + slot3(t + 16);
      if (ROTM & 8) { t += rot4; if (t >= 6) t -= 6; }
      const char* ha = hh + t * 2048 + vl;
      const unsigned sh = sl + w * 4096;
      DMAC(ha, sh, 0); DMAC(ha, sh, 1024);
      DMAC(ha + 49152 - 2048, sh, 2048); DMAC(ha + 49152 - 2048, sh, 3072);
      const char* da = db + t * (64 * 1280);
      const unsigned sb = sl + 16384 + w * 5120;
#pragma unroll
      for (int i = 0; i < 5; i++) DMA(da + vd[i], sb + i * 1024, 0);
    };
    const unsigned ofa = WS_FA + ((8 * (kq & 1) + ((lane >> 2) & 7)) * 16 + 8 * (kq >> 1) + 2 * nt + (lane >> 5)) * 128 + (lane & 3) * 8;
    const unsigned ofh = WS_FH + (mi * 16 + 4 * kq + ((lane >> 2) & 3)) * 128 + (lane & 3) * 8;
#define NEXT(k) do { TW(k); BAR(); TS(k); } while (0)
    const int pc = PCM == 0 ? 10 + (loc & 7) : PCM;
    const int cw = CW == 0 ? 3 + ((loc >> 2) & 1) : CW;
    u32x2 fv;
    LDEP();
    const unsigned t0 = ep[0] * 1664525u + 1013904223u, t1 = 0x40000000u | ((ep[1] + 0x10000u) & 0x3fff0000u);
    WAITV(21); STAMP(2); BAR();
    if (PWM) { issue2(0, 0); WAITV(24); } else WAITV(14);
    NEXT(0);
#pragma nounroll
    for (int t = 1; t < 7; t++) {
      issue1(t + 3);
      if (CWN && t == cw) {
#pragma unroll
        for (int c = 2; c < 2 + CWN; c++) {
          const char* ca = cb + ((c + rot3) & 15) * 128;
#pragma unroll
          for (int j = 0; j < 4; j++) DMA(ca + j * 131072 + va, sm + SLOT + w * 1024, 0);
        }
      }
      if (PWM && t < 3) WAITV(24); else if (CWN && t >= cw && t <= cw + 2) { if (CWN == 2) WAITV(22); else WAITV(18); } else WAITV(14);
      NEXT(t);
    }
    if (PWM) WAITV(7); else { issue2(0, 0); WAITV(17); }
    NEXT(7);
    issue2(1, 1);
    if (PWM) WAITV(10); else WAITV(20);
    NEXT(8);
    issue2(2, 2); WAITV(20);
    NEXT(9);
    {
      int sl = 2;
#pragma nounroll
      for (int t = 10; t < 16; t++) {
        if (t > 10) issue2(t - 8, sl);
        if (t == pc) { issue3a(0, CE); issue3a(1, CE + 16384); }
        if (t == pc || t == pc + 1) WAITV(18); else WAITV(10);
        NEXT(t);
        sl = sl == 2 ? 0 : sl + 1;
      }
    }
    LD8(fv, ws, ofa);
    issue2(8, 2);
    if (pc == 16) { issue3a(0, CE); issue3a(1, CE + 16384); }
    if (pc == 15 || pc == 16) WAITV(19); else WAITV(11);
    NEXT(16);
    issue2(9, 0);
    if (pc == 17) { issue3a(0, CE); issue3a(1, CE + 16384); }
    if (pc >= 16) WAITV(18); else WAITV(10);
    asm volatile("" : "+v"(fv) :: "memory");
    NEXT(17);
    STAMP(3);
    spin(fv, ws, ofa, t0, t1);
    STAMP(4);
    issue3b(0, SLOT); issue3b(1, SLOT + 20480);
    if (pc == 17) WAITV(18); else WAITV(10);
    NEXT(18);
    issue3a(2, 2 * SLOT); issue3b(2, 2 * SLOT + 16384); WAITV(14);
    NEXT(19);
    issue3a(3, 0); issue3b(3, 16384); WAITV(18);
    NEXT(20);
    WAITV(9);
    NEXT(21);
    issue3a(4, SLOT); issue3b(4, SLOT + 16384);
#pragma nounroll
    for (int t = 22; t < 31; t++) {
      const unsigned so = slot3(t - 17);
      issue3a(t - 17, so); issue3b(t - 17, so + 16384);
      WAITV(18);
      NEXT(t);
    }
    LD8(fv, ws, ofh);
#pragma nounroll
    for (int t = 31; t < 33; t++) {
      const unsigned so = slot3(t - 17);
      issue3a(t - 17, so); issue3b(t - 17, so + 16384);
      WAITV(19);
      NEXT(t);
    }
    asm volatile("s_waitcnt vmcnt(18)" : "+v"(fv) :: "memory");
    STAMP(5);
    spin(fv, ws, ofh, t0, t1);
    STAMP(6);
#pragma nounroll
    for (int t = 33; t < 39; t++) { issue4(t - 33); WAITV(18); NEXT(t); }
    WAITV(9); NEXT(39);
    WAITV(0); NEXT(40);
    if (b == 0 && w == 0) {
      const unsigned oa = WS_FA + (lane >> 2) * 128 + (lane & 3) * 8;
#pragma nounroll
      for (int it = 0; it < (1 << 16); it++) {
        u32x2 f[16];
#pragma unroll
        for (int i = 0; i < 16; i++) LD8(f[i], ws, oa + i * 2048);
        asm volatile("s_waitcnt vmcnt(0)" : "+v"(f[0]), "+v"(f[1]), "+v"(f[2]), "+v"(f[3]), "+v"(f[4]), "+v"(f[5]), "+v"(f[6]), "+v"(f[7]),
                     "+v"(f[8]), "+v"(f[9]), "+v"(f[10]), "+v"(f[11]), "+v"(f[12]), "+v"(f[13]), "+v"(f[14]), "+v"(f[15]) :: "memory");
        unsigned m = 0;
#pragma unroll
        for (int i = 0; i < 16; i++) m |= (f[i][0] ^ t0) | (f[i][1] ^ t1);
        if (__builtin_amdgcn_ballot_w64(m != 0) == 0) break;
        __builtin_amdgcn_s_sleep(2);
      }
      st8(wr_, 0, u32x2{t0, t1}, WS_E);
    }
    STAMP(7); STAMP_OUT();
  } else {
    const int w = wid, wr = w >> 1, wc = w & 1;
    const int z = (r >> 1) & 7;
    if (TLBW) {
      const char* tp; unsigned st_, o0_;
      if (w == 0) { tp = (const char*)Wg; o0_ = 0; st_ = 245760; }
      else if (w == 1) { tp = ws; o0_ = 2u << 20; st_ = 2u << 20; }
      else if (w == 2) { tp = ws; o0_ = 18u << 20; st_ = 2u << 20; }
      else { tp = (const char*)Wu; o0_ = 0; st_ = 245760; }
      const char* tq = w == 0 ? (const char*)Wd + kq * 491520 : w == 1 ? (const char*)((kq >> 1) ? Ci : Cr) + mi * 524288 : w == 2 ? (const char*)out + mi * 163840 : (const char*)Wd + kq * 491520 + 245760;
      unsigned d_[10];
#pragma unroll
      for (int i = 0; i < 8; i++) asm volatile("s_load_dword %0, %1, %2" : "=s"(d_[i]) : "s"(tp), "s"(o0_ + i * st_) : "memory");
      asm volatile("s_load_dword %0, %1, 0" : "=s"(d_[8]) : "s"(tq) : "memory");
      asm volatile("s_load_dword %0, %1, %2" : "=s"(d_[9]) : "s"(tq), "s"(w == 1 ? 524284u : 163836u) : "memory");
      asm volatile("s_waitcnt lgkmcnt(0)" : "+s"(d_[0]), "+s"(d_[1]), "+s"(d_[2]), "+s"(d_[3]), "+s"(d_[4]), "+s"(d_[5]), "+s"(d_[6]), "+s"(d_[7]), "+s"(d_[8]), "+s"(d_[9]) :: "memory");
    }
    LDEP();
    const unsigned t0 = ep[0] * 1664525u + 1013904223u, t1 = 0x40000000u | ((ep[1] + 0x10000u) & 0x3fff0000u);
    const int grp = mi * 4 + nt;
    const unsigned l16 = lane * 16;
    constexpr int L1 = 2 * P1S, L2 = 2 * SLOT;
    unsigned ax0 = sm + P1B + (8 * (r >> 2) + (r & 3)) * 128 + ((q ^ z) << 4) + 4096 * w, ax1 = ax0 ^ 64;
    unsigned ah0 = sm + P1B + 16384 + r * 128 + ((q ^ z) << 4), ah1 = ah0 ^ 64, ag0 = sm + P1B + 16384 + 10240 + (q << 4);
    unsigned ax0h = ax0 + L1, ax1h = ax1 + L1, ah0h = ah0 + L1, ah1h = ah1 + L1, ag0h = ag0 + L1;
    OPQ(ax0); OPQ(ax1); OPQ(ah0); OPQ(ah1); OPQ(ag0); OPQ(ax0h); OPQ(ax1h); OPQ(ah0h); OPQ(ah1h); OPQ(ag0h);
    f32x4 c1[2][6];
    bf16x8 p1a[2][2], p1b[2][6];
    auto rd1 = [&](int T, int h, int k) INL {
      const int so = ((T + 2) & 3) * P1S;
      if (k == 0 || k == 2) p1a[h][k >> 1] = lds128(la(h ? ax1 : ax0, h ? ax1h : ax0h, so + (k >> 1) * 512, L1));
      else if (k < 7) { const int j = k == 1 ? 0 : k - 2; p1b[h][j] = lds128(la(h ? ah1 : ah0, h ? ah1h : ah0h, so + j * 2048, L1)); }
      else if (k == 7) p1b[h][5] = lds128(la(ag0, ag0h, so + h * 64, L1));
      SB0();
    };
    unsigned bx0 = sm + (64 * wr + r) * 128 + ((q ^ z) << 4), bx1 = bx0 ^ 64;
    const int qq = r >> 2, sg = ((qq >> 1) & 1) | ((q & 1) << 1);
    unsigned tA0 = sm + (8 * q + qq) * 128 + 8 * (lane & 3) + (((2 * wc) ^ sg) << 5) + 16384, tA1 = tA0 ^ 32;
    unsigned bx0h = bx0 + L2, bx1h = bx1 + L2, tA0h = tA0 + L2, tA1h = tA1 + L2;
    OPQ(bx0); OPQ(bx1); OPQ(tA0); OPQ(tA1); OPQ(bx0h); OPQ(bx1h); OPQ(tA0h); OPQ(tA1h);
    f32x4 cg[12], cu[12];
    bf16x8 xx[2][4]; bf16x4 wl[2][6], wh[2][6];
    auto rd2 = [&](int t, int h, int k) INL {
      const int so = (t % 3) * SLOT;
      if (k >= 2 && k < 6) xx[h][k - 2] = lds128(la(h ? bx1 : bx0, h ? bx1h : bx0h, so + (k - 2) * 2048, L2));
      else if (k < 16) {
        const int kk = k < 2 ? k : k - 4, f = kk >> 1;
        const bool s1 = f == 2 || f == 3 || f == 5;
        const int off = so + h * 4096 + (kk & 1) * 512 + (f >= 4 ? 16384 : (f & 1) * 8192);
        const bf16x4 v = ldstr(la(s1 ? tA1 : tA0, s1 ? tA1h : tA0h, off, L2));
        if (kk & 1) wh[h][f] = v; else wl[h][f] = v;
      }
      SB0();
    };
    unsigned ab0 = sm + wc * 5120 + lane * 16 + 16384, ab0h = ab0 + L2;
    OPQ(ab0); OPQ(ab0h);
    f32x4 acc[4][5];
    bf16x8 fa[2][4], fb[2][5]; bf16x4 dl[2][5], dh_[2][5];
    auto sa3 = [&](int t) INL { return t < 2 ? CE + 16384 * t : (int)((0x3102u >> (4 * ((t + 2) & 3))) & 3) * SLOT; };
    auto sb3 = [&](int t) INL { return t < 2 ? SLOT - 16384 + 20480 * t : sa3(t); };
    auto rd3 = [&](int t, int h, int k) INL {
      if (k >= 1 && k < 5) fa[h][k - 1] = lds128(la(h ? bx1 : bx0, h ? bx1h : bx0h, sa3(t) + (k - 1) * 2048, L2));
      else if (k < 9) { const int j = k == 0 ? 0 : k - 4; fb[h][j] = lds128(la(ab0, ab0h, sb3(t) + h * 10240 + j * 1024, L2)); }
      SB0();
    };
    BAR();
    STAMP(1);
    UNR for (int k = 0; k < 8; k++) rd1(0, 0, k);
    LGKM0(); SB0();
    SF(t, 10)
      UNR for (int k = 0; k < 12; k++) {
        if (t == 0) MFMAZ(p1a[0][k & 1], p1b[0][k >> 1], c1[k & 1][k >> 1]); else MFMAT(p1a[0][k & 1], p1b[0][k >> 1], c1[k & 1][k >> 1]);
        rd1(t, 1, k);
        SB0();
      }
      LGKM0(); BAR(); SB0();
      UNR for (int k = 0; k < 12; k++) {
        MFMAT(p1a[1][k & 1], p1b[1][k >> 1], c1[k & 1][k >> 1]);
        if (t < 9) rd1(t + 1, 0, k); else rd2(0, 0, k);
        SB0();
      }
      if (t == 9) { UNR for (int k = 12; k < 16; k++) rd2(0, 0, k); } else LGKM0();
      SB0();
    SE
    STAMP(2);
    float gv[4], iog[4];
    u32x4 abp[5], glp;
    u32x2 gp[12];
    const unsigned dab = WS_AB + ((((nj >> 3) * 64 + 4 * mi + w) * 40) + 5 * (nj & 7)) * 1024;
    const unsigned go = WS_G + b * 512 + (64 * wr + r) * 4;
    SF(t, 10)
      SF(h, 2)
        UNR for (int k = 0; k < 24; k++) {
          const bool lastH = t == 9 && h == 1;
          const int f = lastH ? (k < 12 ? 2 * (k >> 2) : 2 * ((k - 12) >> 2) + 1) : k >> 2;
          const int i = k & 3, n = 4 * (f >> 1) + i;
          const bf16x8 wf = cat(wl[h][f], wh[h][f]);
          if (t == 0 && h == 0) { if (f & 1) MFMAZ(wf, xx[h][i], cu[n]); else MFMAZ(wf, xx[h][i], cg[n]); }
          else { if (f & 1) MFMAT(wf, xx[h][i], cu[n]); else MFMAT(wf, xx[h][i], cg[n]); }
          if (h == 0) rd2(t, 1, k); else if (t < 9) rd2(t + 1, 0, k); else rd3(0, 0, k);
          if (t == 0 && h == 0 && k >= 4) { const int e = k - 4, j = e >> 2, c = e & 3; abp[j][c] = pk2(c1[c >> 1][j][2 * (c & 1)], c1[c >> 1][j][2 * (c & 1) + 1]); }
          if (t == 0 && h == 1) {
            if (k < 4) glp[k] = __float_as_uint((r & 1) ? c1[1][5][k] : c1[0][5][k]);
            if (!(XP & 2) && k >= 16 && k < 21) st16(wr_, l16 + ((k - 16) & 3) * 1024, abp[k - 16], dab + ((k - 16) >> 2) * 4096);
            if (k == 21) st16p(wr_, WS_G + (8 * q + 4 * (r & 1)) * 4, glp, b * 512 + w * 128);
          }
          if (t == PUB1 + 2 && k >= 16) {
            const int i2 = 2 * h + ((k - 16) >> 2), e = (k - 16) & 3;
            if (e == 0) gv[i2] = gv[i2] * 1.44269504088896341f;
            if (e == 1) gv[i2] = __builtin_amdgcn_exp2f(gv[i2]);
            if (e == 2) iog[i2] = fminf(gv[i2] + 1.f, 3.0e38f);
            if (e == 3) gv[i2] = __builtin_amdgcn_rcpf(iog[i2]);
          }
          if (t == PUB1 + 3 && h == 0 && k >= 20) gv[k - 20] = kq >= 2 ? gv[k - 20] - 1.f : 1.f - gv[k - 20];
          if (lastH && k >= 10 && k < 22) gp[k - 10] = u32x2{pkh(cg[k - 10][0], cg[k - 10][1]), pkh(cg[k - 10][2], cg[k - 10][3])};
          SB0();
        }
        if (h == 0) {
          if (t == PUB1) { WAITV(0); st8(wr_, 0, u32x2{t0, t1}, WS_FA + (mi * 16 + nj) * 128 + w * 8); }
          if (t == PUB1 + 1) asm volatile("s_waitcnt vmcnt(0)" : "+v"(gv[0]), "+v"(gv[1]), "+v"(gv[2]), "+v"(gv[3]) :: "memory");
          LGKM0(); BAR();
          if (t == PUB1) {
            UNR for (int i = 0; i < 4; i++) asm volatile("global_load_dword %0, %1, %2" : "=v"(gv[i]) : "v"(go + 64 * (i ^ kq)), "s"(ws) : "memory");
          }
        } else LGKM0();
        SB0();
      SE
    SE
    STAMP(3);
    unsigned ah4 = sm + wr * 8192 + lane * 16;
    unsigned tde = sm + 16384 + (8 * q + (r >> 2)) * 320 + 8 * (lane & 3) + 160 * wc + ((q & 1) ? (wc ? -32 : 32) : 0);
    unsigned tdo = sm + 16384 + (8 * q + (r >> 2)) * 320 + 8 * (lane & 3) + 160 * wc + ((q & 1) ? (wc ? 32 : -32) : 0);
    unsigned ah4h, tdeh, tdoh;
    auto rd4 = [&](int t, int h, int k) INL {
      const int so = sa3(t + 16);
      if (k >= 2 && k < 6) fa[h][k - 2] = lds128(la(ah4, ah4h, so + h * 1024 + (k - 2) * 2048, L2));
      else if (k < 14) {
        const int kk = k < 2 ? k : k - 4, j = kk >> 1;
        const bf16x4 v = ldstr(la((j & 1) ? tdo : tde, (j & 1) ? tdoh : tdeh, so + h * 10240 + 32 * j + (kk & 1) * 1280, L2));
        if (kk & 1) dh_[h][j] = v; else dl[h][j] = v;
      }
      SB0();
    };
    {
      const int fa16 = (nj & 1) ? 2 : 0, fb16 = (nj & 1) ? 0 : 4;
      const unsigned dh = WS_H + ((8 * mi + 4 * wr) * 48 + 3 * nj) * 1024, dhv = (q & 1) * 49152 + ((q >> 1) * 16 + r) * 16;
      const float nl2e = -1.44269504088896341f;
      float ta[4], tb[4]; u32x2 e0, e1; unsigned s0[2], s1[2];
      auto mix = [&](int n, int c) INL { return (c & 1) ? mixhi(gp[n][c >> 1], nl2e) : mixlo(gp[n][c >> 1], nl2e); };
      SF(t, 16)
        UNR for (int k = 0; k < 20; k++) {
          if (t == 0) MFMAZ(fb[0][k >> 2], fa[0][k & 3], acc[k & 3][k >> 2]); else MFMAT(fb[0][k >> 2], fa[0][k & 3], acc[k & 3][k >> 2]);
          rd3(t, 1, k);
          const int n = 2 * t;
          if (t >= 1 && t < 7) {
            if (k == 0) e1[1] = pk2(tb[2], tb[3]);
            if (k == 4) { auto s = __builtin_amdgcn_permlane16_swap(e0[0], e1[0], false, false); s0[0] = s[0]; s0[1] = s[1]; }
            if (k == 5) { auto s = __builtin_amdgcn_permlane16_swap(e0[1], e1[1], false, false); s1[0] = s[0]; s1[1] = s[1]; }
            if (k == 6) {
              const int c = (t - 1) >> 1, blk = c < 2 ? fa16 + 2 * wc + c : fb16 + wc;
              st16(wr_, dhv, u32x4{s0[0], s1[0], s0[1], s1[1]}, dh + (blk >> 1) * 1024 + (blk & 1) * 512 + ((t - 1) & 1) * 98304);
            }
          }
          if (t < 6) {
            if (k < 4) ta[k] = mix(n, k);
            if (k == 7 || k == 8) tb[k - 7] = mix(n + 1, k - 7);
            if (k >= 9 && k < 13) ta[k - 9] = __builtin_amdgcn_exp2f(ta[k - 9]);
            if (k == 13 || k == 14) { const int c = 2 * (k - 13); ta[c] = ffma(ta[c], iog[n & 3], iog[n & 3]); ta[c + 1] = ffma(ta[c + 1], iog[n & 3], iog[n & 3]); }
            if (k >= 15 && k < 19) ta[k - 15] = __builtin_amdgcn_rcpf(ta[k - 15]);
            if (k == 19) { ta[0] = fmul(ta[0], cu[n][0]); ta[1] = fmul(ta[1], cu[n][1]); }
          }
          if (PRE == 1 && k == 16 && t >= PRET && t < PRET + 9) {
            const int e = t - PRET, i1 = 1 + e / 3;
            st16(wr_, l16 + (e % 3) * 1024, u32x4{0, 0, 0, 0}, WS_P + (((grp * 4 + (kq ^ i1)) * 4 + i1) * 4 + w) * 3072);
          }
          if (PRE == 2 && k == 16 && t >= PRET && t < PRET + 3) {
            const int i1 = 1 + t - PRET;
            __builtin_amdgcn_raw_buffer_store_b32(0u, wr_, (lane < 24 ? lane : 23) * 128, WS_P + (((grp * 4 + (kq ^ i1)) * 4 + i1) * 4 + w) * 3072, 16);
          }
          if (t == 12 && k >= 12 && k < 15) {
            if (k == 12) { OPQ(ah4); ah4h = ah4 + L2; OPQ(ah4h); }
            if (k == 13) { OPQ(tde); tdeh = tde + L2; OPQ(tdeh); }
            if (k == 14) { OPQ(tdo); tdoh = tdo + L2; OPQ(tdoh); }
          }
          SB0();
        }
        if (t == PUB2) { WAITV(0); st8(wr_, 0, u32x2{t0, t1}, WS_FH + (mi * 16 + nj) * 128 + w * 8); }
        LGKM0(); BAR(); SB0();
        UNR for (int k = 0; k < 20; k++) {
          MFMAT(fb[1][k >> 2], fa[1][k & 3], acc[k & 3][k >> 2]);
          const int n = 2 * t + 1;
          if (t < 6) {
            const int kr = k < 6 ? k : k == 10 ? 6 : k == 11 ? 7 : k == 14 ? 8 : 99;
            rd3(t + 1, 0, kr);
            if (k < 2) ta[2 + k] = fmul(ta[2 + k], cu[n - 1][2 + k]);
            if (k == 2) e0[0] = pk2(ta[0], ta[1]);
            if (k == 3) e0[1] = pk2(ta[2], ta[3]);
            if (k == 4 || k == 5) tb[k - 2] = mix(n, k - 2);
            if (k >= 6 && k < 10) tb[k - 6] = __builtin_amdgcn_exp2f(tb[k - 6]);
            if (k == 10 || k == 11) tb[k - 10] = ffma(tb[k - 10], iog[n & 3], iog[n & 3]);
            if (k == 12 || k == 13) tb[k - 12] = __builtin_amdgcn_rcpf(tb[k - 12]);
            if (k == 14) tb[2] = ffma(tb[2], iog[n & 3], iog[n & 3]);
            if (k == 15) { tb[3] = ffma(tb[3], iog[n & 3], iog[n & 3]); tb[0] = fmul(tb[0], cu[n][0]); }
            if (k == 16 || k == 17) tb[k - 14] = __builtin_amdgcn_rcpf(tb[k - 14]);
            if (k == 18) { tb[1] = fmul(tb[1], cu[n][1]); tb[2] = fmul(tb[2], cu[n][2]); }
            if (k == 19) { tb[3] = fmul(tb[3], cu[n][3]); e1[0] = pk2(tb[0], tb[1]); }
          } else if (t < 15) rd3(t + 1, 0, k); else rd4(0, 0, k);
          SB0();
        }
        if (t >= 6) LGKM0();
        SB0();
      SE
      STAMP(5);
      UNR for (int j = 0; j < 5; j++) {
        UNR for (int i = 0; i < 4; i++) acc[i][j] *= gv[i];
        SB0();
      }
    }
    SF(t, 5)
      SF(h, 2)
        UNR for (int k = 0; k < 20; k++) {
          MFMAT(cat(dl[h][k >> 2], dh_[h][k >> 2]), fa[h][k & 3], acc[k & 3][k >> 2]);
          if (h == 0) rd4(t, 1, k); else rd4(t + 1, 0, k);
          SB0();
        }
        LGKM0();
        if (h == 0) BAR();
        SB0();
      SE
    SE
    unsigned pv;
    const unsigned pi = WS_P + ((grp * 4 + kq) * 16 + w) * 3072;
    const unsigned ofp = pi + (1 + (lane & 3) % 3) * 12288 + 3068;
    {
      u32x4 o0, o1, o2;
      const float tf = __uint_as_float(t1);
      UNR for (int k = 0; k < 40; k++) {
        const int h = k < 15 ? 0 : k < 30 ? 1 : (k - 30) / 5;
        const int i = k < 15 ? 1 + k % 3 : k < 30 ? 1 + (k - 15) / 5 : 0;
        const int j = k < 15 ? k / 3 : k % 5;
        MFMAT(cat(dl[h][j], dh_[h][j]), fa[h][i], acc[i][j]);
        if (k < 14) rd4(5, 1, k);
        if (k == 14) LGKM0();
        if (POLL && POLL != 3 && k == POLLK) LD4(pv, ws, ofp);
        if (k >= 23 && k < 38) {
          const int i1 = 1 + (k - 23) / 5, j1 = (k - 23) % 5;
          const f32x4 v = acc[i1][j1];
          const unsigned po = WS_P + (((grp * 4 + (kq ^ i1)) * 4 + i1) * 4 + w) * 3072;
          if (j1 == 0) { o0[0] = pk2(v[2], v[3]); o0[3] = pk2(v[0], tf); }
          if (j1 == 1) { o0[1] = pk2(v[0], v[1]); o0[2] = pk2(v[2], v[3]); st16(wr_, l16, o0, po); }
          if (j1 == 2) { o1[0] = pk2(v[0], v[1]); o1[1] = pk2(v[2], v[3]); }
          if (j1 == 3) { o1[2] = pk2(v[0], v[1]); o1[3] = pk2(acc[i1][0][1], tf); st16(wr_, l16 + 1024, o1, po); }
          if (j1 == 4) { o2[0] = pk2(acc[i1][3][2], acc[i1][3][3]); o2[1] = pk2(v[0], v[1]); o2[2] = pk2(v[2], v[3]); o2[3] = t0; st16(wr_, l16 + 2048, o2, po); }
        }
        SB0();
      }
    }
    STAMP(7);
    {
      u32x4 p0[3], p1[3], p2[3];
      if (POLL && POLL != 3) {
        asm volatile("s_waitcnt vmcnt(9)" : "+v"(pv) :: "memory");
#pragma nounroll
        for (int it = 0; it < (1 << 16); it++) {
          if (__builtin_amdgcn_ballot_w64(pv != t0) == 0) break;
          if (POLL > 1) __builtin_amdgcn_s_sleep(POLL);
          LD4(pv, ws, ofp);
          asm volatile("s_waitcnt vmcnt(0)" : "+v"(pv) :: "memory");
        }
        STAMP(9);
      }
#pragma nounroll
      for (int it = 0; it < (1 << 16); it++) {
#pragma unroll
        for (int s = 0; s < 3; s++) {
          const unsigned ps = pi + l16 + (s + 1) * 12288;
          LD16(p0[s], ws, ps, 0); LD16(p1[s], ws, ps, 1024); LD16(p2[s], ws, ps, 2048);
        }
        asm volatile("s_waitcnt vmcnt(0)" : "+v"(p0[0]), "+v"(p0[1]), "+v"(p0[2]), "+v"(p1[0]), "+v"(p1[1]), "+v"(p1[2]), "+v"(p2[0]), "+v"(p2[1]), "+v"(p2[2]) :: "memory");
        unsigned m = 0, m2 = 0;
#pragma unroll
        for (int s = 0; s < 3; s++) { m |= (p0[s][3] ^ t1) | (p1[s][3] ^ t1); m2 |= p2[s][3] ^ t0; }
        if (it == 0) STAMP(8);
        if (__builtin_amdgcn_ballot_w64(((m >> 16) | m2) != 0) == 0) break;
        __builtin_amdgcn_s_sleep(2);
      }
      STAMP(10);
      f32x4 y[5];
#pragma unroll
      for (int j = 0; j < 5; j++) y[j] = acc[0][j];
#pragma unroll
      for (int s = 0; s < 3; s++) {
        y[0][0] += bflo(p0[s][3]); y[0][1] += bflo(p1[s][3]); y[0][2] += bflo(p0[s][0]); y[0][3] += bfhi(p0[s][0]);
        y[1][0] += bflo(p0[s][1]); y[1][1] += bfhi(p0[s][1]); y[1][2] += bflo(p0[s][2]); y[1][3] += bfhi(p0[s][2]);
        y[2][0] += bflo(p1[s][0]); y[2][1] += bfhi(p1[s][0]); y[2][2] += bflo(p1[s][1]); y[2][3] += bfhi(p1[s][1]);
        y[3][0] += bflo(p1[s][2]); y[3][1] += bfhi(p1[s][2]); y[3][2] += bflo(p2[s][0]); y[3][3] += bfhi(p2[s][0]);
        y[4][0] += bflo(p2[s][1]); y[4][1] += bfhi(p2[s][1]); y[4][2] += bflo(p2[s][2]); y[4][3] += bfhi(p2[s][2]);
      }
      char* dst = (char*)out + (size_t)(128 * mi + 64 * wr + 16 * kq + r) * 1280 + (160 * nt + 80 * wc) * 2 + q * 8;
#pragma unroll
      for (int j = 0; j < 5; j++) *(GAS u32x2*)(dst + j * 32) = u32x2{pk2(y[j][0], y[j][1]), pk2(y[j][2], y[j][3])};
      STAMP(11); STAMP_OUT();
    }
  }
}

extern "C" void kernel_launch(void* const* d_in, const int* in_sizes, int n_in, void* d_out, int out_size, void* d_ws, size_t ws_size,
                              hipStream_t stream) {
  (void)hipFuncSetAttribute((const void*)kf, hipFuncAttributeMaxDynamicSharedMemorySize, LDS_BYTES);
  kf<<<256, 512, LDS_BYTES, stream>>>((const u16*)d_in[0], (const u16*)d_in[3], (const u16*)d_in[4], (const u16*)d_in[6], (const u16*)d_in[7],
                                      (const u16*)d_in[8], (const u16*)d_in[9], (const u16*)d_in[1], (const u16*)d_in[2], (char*)d_ws, (u16*)d_out);
}
```
